```python
import torch

def _softmax(x, axis=-1):
    return torch.softmax(x, dim=axis)
def _gelu(x):
    return torch.nn.functional.gelu(x, approximate="tanh")
def _silu(x):
    return torch.nn.functional.silu(x)
def _sigmoid(x):
    return torch.sigmoid(x)
def _layernorm(x, axis=-1, eps=1e-5):
    m = x.mean(dim=axis, keepdim=True)
    v = x.var(dim=axis, keepdim=True, unbiased=False)
    return (x - m) / torch.sqrt(v + eps)
def _scatter_add(tgt, idx, src, axis=0):
    return tgt.clone().index_add_(axis, idx, src)
def _segment_sum(src, seg, n):
    out = torch.zeros((n,) + src.shape[1:], dtype=src.dtype)
    return out.index_add_(0, seg, src)
def _pad(x, pw):
    flat = tuple(v for pair in reversed(pw) for v in pair)
    return torch.nn.functional.pad(x, flat)

kernel_name = 'gated_mix__sliding_local_dilated__linear_kernel_hedgehog__prompt'

def setup_inputs():

    _w = (lambda a: (lambda t: t.to(torch.bfloat16) if t.is_floating_point() else t)(torch.as_tensor(a)))
    def _A_setup():
        B, S, D = 1, 2048, 448
        return {
            "x": _w((lambda *s: torch.randn(*s, dtype=torch.bfloat16) * 0.05)(B, S, D)),
            "wqkv": _w((lambda *s: torch.randn(*s, dtype=torch.bfloat16) * 0.8)(D, 3 * D)),
            "wo": _w((lambda *s: torch.randn(*s, dtype=torch.bfloat16) * 0.05)(D, D)),
        }
    _A_in = _A_setup()

    def _B_setup():
        B, S, D, H = 1, 2048, 448, 16
        Dh = D // H
        return {
            "x": _w((lambda *s: torch.randn(*s, dtype=torch.bfloat16) * 0.05)(B, S, D)),
            "wqkv": _w((lambda *s: torch.randn(*s, dtype=torch.bfloat16) * 1.6)(D, 3 * D)),
            "w_phi": _w((lambda *s: torch.randn(*s, dtype=torch.bfloat16) * 1.6)(H, Dh, Dh) * (Dh ** -0.5)),
            "wo": _w((lambda *s: torch.randn(*s, dtype=torch.bfloat16) * 0.2)(D, D)),
        }
    _B_in = _B_setup()

    return {
        "A_x": _A_in["x"],
        "A_wqkv": _A_in["wqkv"],
        "A_wo": _A_in["wo"],
        "B_x": _B_in["x"],
        "B_wqkv": _B_in["wqkv"],
        "B_w_phi": _B_in["w_phi"],
        "B_wo": _B_in["wo"],
        "gate_w": _w((lambda *s: torch.randn(*s, dtype=torch.bfloat16) * 0.05)(448) * 16.0),
    }

def reference(A_x, A_wqkv, A_wo, B_x, B_wqkv, B_w_phi, B_wo, gate_w):
    def _A_ref(x, wqkv, wo):
        B, S, D = x.shape
        H = 16; Dh = D // H
        W, dil = 8, 2
        qkv = (lambda x, *d: x.permute(*d))((x @ wqkv).reshape(B, S, 3, H, Dh), 2, 0, 3, 1, 4)
        q, k, v = qkv[0], qkv[1], qkv[2]
        scale = Dh ** -0.5
        pos = torch.arange(S)
        off = torch.arange(W) * dil
        idx = pos[:, None] - off[None, :]
        mask = idx >= 0
        idx_c = torch.where(mask, idx, 0).reshape(-1)
        kg = k[:, :, idx_c, :].reshape(B, H, S, W, Dh)
        vg = v[:, :, idx_c, :].reshape(B, H, S, W, Dh)
        logits = torch.einsum("bhsd,bhswd->bhsw", q, kg) * scale
        logits = torch.where(mask[None, None, :, :], logits, -1e9)
        attn = _softmax(logits, axis=-1)
        o = torch.einsum("bhsw,bhswd->bhsd", attn, vg)
        return (lambda x, *d: x.permute(*d))(o, 0, 2, 1, 3).reshape(B, S, D) @ wo

    def _B_ref(x, wqkv, w_phi, wo):
        B, S, D = x.shape
        H = 16; Dh = D // H
        qkv = (lambda x, *d: x.permute(*d))((x @ wqkv).reshape(B, S, 3, H, Dh), 2, 0, 3, 1, 4)
        q, k, v = qkv[0], qkv[1], qkv[2]
        pq = torch.einsum("bhsd,hdf->bhsf", q, w_phi)
        pk = torch.einsum("bhsd,hdf->bhsf", k, w_phi)
        phi_q = torch.concatenate([_softmax(pq, axis=-1), _softmax(-pq, axis=-1)], axis=-1)
        phi_k = torch.concatenate([_softmax(pk, axis=-1), _softmax(-pk, axis=-1)], axis=-1)
        kv = torch.einsum("bhsf,bhsd->bhfd", phi_k, v)
        z  = torch.einsum("bhsf->bhf", phi_k)
        num = torch.einsum("bhqf,bhfd->bhqd", phi_q, kv)
        den = torch.einsum("bhqf,bhf->bhq", phi_q, z)[..., None] + 1e-6
        o = num / den
        return (lambda x, *d: x.permute(*d))(o, 0, 2, 1, 3).reshape(B, S, D) @ wo

    x = A_x
    g = _sigmoid((x * gate_w).sum(-1, keepdims=True))
    a = _A_ref(x, A_wqkv, A_wo)
    b = _B_ref(x, B_wqkv, B_w_phi, B_wo)
    return g * a + (1 - g) * b
```

```cpp
#include <hip/hip_runtime.h>
#include <hip/hip_bf16.h>

#define S_ 2048
#define D_ 448
#define H_ 16
#define DH 28

__device__ __forceinline__ float bf2f(unsigned short u) { return __uint_as_float(((unsigned)u) << 16); }

__global__ void k_qkv(const unsigned short* x, const unsigned short* wA, const unsigned short* wB, float* qkv) {
    int n = blockIdx.x * 64 + threadIdx.x;
    int s = blockIdx.y;
    if (n >= 2 * 3 * D_) return;
    const unsigned short* w = n < 3 * D_ ? wA : wB;
    int nn = n < 3 * D_ ? n : n - 3 * D_;
    float acc = 0.f;
    for (int k = 0; k < D_; ++k) acc += bf2f(x[s * D_ + k]) * bf2f(w[k * 3 * D_ + nn]);
    qkv[(size_t)s * 6 * D_ + n] = acc;
}

__global__ void k_phi(const float* qkv, const unsigned short* wphi, float* phi) {
    int idx = blockIdx.x * 64 + threadIdx.x;
    int h = idx & 15, which = (idx >> 4) & 1, s = idx >> 5;
    const float* q = qkv + (size_t)s * 6 * D_ + 3 * D_ + which * D_ + h * DH;
    float* o = phi + (size_t)idx * 56;
    float mx = -1e30f, mn = 1e30f;
    for (int f = 0; f < DH; ++f) {
        float a = 0.f;
        for (int d = 0; d < DH; ++d) a += q[d] * bf2f(wphi[(h * DH + d) * DH + f]);
        o[f] = a;
        mx = fmaxf(mx, a); mn = fminf(mn, a);
    }
    float sp = 0.f, sn = 0.f;
    for (int f = 0; f < DH; ++f) {
        float a = o[f];
        float ep = __expf(a - mx), en = __expf(mn - a);
        o[f] = ep; o[DH + f] = en; sp += ep; sn += en;
    }
    for (int f = 0; f < DH; ++f) { o[f] /= sp; o[DH + f] /= sn; }
}

__global__ void k_kv(const float* qkv, const float* phi, float* kv) {
    int idx = blockIdx.x * 64 + threadIdx.x;
    if (idx >= H_ * 56 * 29) return;
    int d = idx % 29, f = (idx / 29) % 56, h = idx / (29 * 56);
    float acc = 0.f;
    for (int s = 0; s < S_; ++s) {
        float p = phi[((size_t)s * 32 + 16 + h) * 56 + f];
        float v = d < 28 ? qkv[(size_t)s * 6 * D_ + 3 * D_ + 2 * D_ + h * DH + d] : 1.f;
        acc += p * v;
    }
    kv[idx] = acc;
}

__global__ void k_o(const unsigned short* x, const unsigned short* gw, const float* qkv, const float* phi, const float* kv, float* o) {
    int idx = blockIdx.x * 64 + threadIdx.x;
    int h = idx & 15, s = idx >> 4;
    float gs = 0.f;
    for (int k = 0; k < D_; ++k) gs += bf2f(x[s * D_ + k]) * bf2f(gw[k]);
    float g = 1.f / (1.f + __expf(-gs));
    const float* q = qkv + (size_t)s * 6 * D_ + h * DH;
    float lg[8]; float mx = -1e30f;
#pragma unroll
    for (int w = 0; w < 8; ++w) {
        int t = s - 2 * w;
        float a = -1e9f;
        if (t >= 0) {
            a = 0.f;
            const float* k = qkv + (size_t)t * 6 * D_ + D_ + h * DH;
            for (int d = 0; d < DH; ++d) a += q[d] * k[d];
            a *= 0.18898223650461362f;
        }
        lg[w] = a; mx = fmaxf(mx, a);
    }
    float sum = 0.f;
#pragma unroll
    for (int w = 0; w < 8; ++w) { lg[w] = __expf(lg[w] - mx); sum += lg[w]; }
    for (int d = 0; d < DH; ++d) {
        float a = 0.f;
#pragma unroll
        for (int w = 0; w < 8; ++w) {
            int t = s - 2 * w; if (t < 0) t = 0;
            a += lg[w] * qkv[(size_t)t * 6 * D_ + 2 * D_ + h * DH + d];
        }
        o[(size_t)s * 896 + h * DH + d] = g * a / sum;
    }
    const float* pq = phi + ((size_t)s * 32 + h) * 56;
    float den = 1e-6f;
    for (int f = 0; f < 56; ++f) den += pq[f] * kv[(h * 56 + f) * 29 + 28];
    for (int d = 0; d < DH; ++d) {
        float a = 0.f;
        for (int f = 0; f < 56; ++f) a += pq[f] * kv[(h * 56 + f) * 29 + d];
        o[(size_t)s * 896 + 448 + h * DH + d] = (1.f - g) * a / den;
    }
}

__global__ void k_out(const float* o, const unsigned short* woA, const unsigned short* woB, __hip_bfloat16* out) {
    int n = blockIdx.x * 64 + threadIdx.x;
    int s = blockIdx.y;
    float acc = 0.f;
    for (int k = 0; k < D_; ++k) acc += o[(size_t)s * 896 + k] * bf2f(woA[k * D_ + n]);
    for (int k = 0; k < D_; ++k) acc += o[(size_t)s * 896 + 448 + k] * bf2f(woB[k * D_ + n]);
    out[(size_t)s * D_ + n] = __float2bfloat16(acc);
}

extern "C" void kernel_launch(void* const* d_in, const int* in_sizes, int n_in,
                              void* d_out, int out_size, void* d_ws, size_t ws_size,
                              hipStream_t stream) {
    const unsigned short* x = (const unsigned short*)d_in[0];
    const unsigned short* wA = (const unsigned short*)d_in[1];
    const unsigned short* woA = (const unsigned short*)d_in[2];
    const unsigned short* wB = (const unsigned short*)d_in[4];
    const unsigned short* wphi = (const unsigned short*)d_in[5];
    const unsigned short* woB = (const unsigned short*)d_in[6];
    const unsigned short* gw = (const unsigned short*)d_in[7];
    float* qkv = (float*)d_ws;
    float* phi = qkv + (size_t)S_ * 6 * D_;
    float* kv = phi + (size_t)S_ * 32 * 56;
    float* o = kv + 32768;
    k_qkv<<<dim3(42, S_), 64, 0, stream>>>(x, wA, wB, qkv);
    k_phi<<<S_ * 32 / 64, 64, 0, stream>>>(qkv, wphi, phi);
    k_kv<<<(H_ * 56 * 29 + 63) / 64, 64, 0, stream>>>(qkv, phi, kv);
    k_o<<<S_ * 16 / 64, 64, 0, stream>>>(x, gw, qkv, phi, kv, o);
    k_out<<<dim3(7, S_), 64, 0, stream>>>(o, woA, woB, (__hip_bfloat16*)d_out);
}
```

```cpp
#include <hip/hip_runtime.h>
#include <stdint.h>

typedef short bf16x8 __attribute__((ext_vector_type(8)));
typedef __bf16 bf16x8_t __attribute__((ext_vector_type(8)));
typedef __bf16 bf16x2_t __attribute__((ext_vector_type(2)));
typedef short s16x4 __attribute__((ext_vector_type(4)));
typedef float f32x2 __attribute__((ext_vector_type(2)));
typedef float f32x4 __attribute__((ext_vector_type(4)));
typedef float f32x16 __attribute__((ext_vector_type(16)));
typedef unsigned u32x2 __attribute__((ext_vector_type(2)));
typedef unsigned u32x4 __attribute__((ext_vector_type(4)));
#define LAS __attribute__((address_space(3)))
#define DI __device__ __forceinline__
typedef __amdgpu_buffer_rsrc_t rsrc_t;

template <int N> struct ic { static constexpr int value = N; };

static constexpr unsigned WS_G = 0;
static constexpr unsigned WS_FLAG = 4096;
static constexpr unsigned WS_L2 = 65536;
static constexpr unsigned WS_L1 = 1u << 20;
static constexpr unsigned WS_OF = 5u << 20;
static constexpr unsigned WS_END = 9u << 20;

static constexpr int A_SLOT = 43008, A_HALO = 16384, A_W = 18432, A_XCH = 129024;
static constexpr int B_SLOT = 32768, B_W = 16384, B_WQ = 98304, B_PHIW = 155648, B_PHI = 32768, B_VF = 65536;
static constexpr int LDS_BYTES = 163840;
static constexpr int SPIN_CAP = 1 << 18;
#define L2SLEEP 4

DI f32x16 mfma(u32x4 a, u32x4 b, f32x16 c) {
    return __builtin_amdgcn_mfma_f32_32x32x16_bf16(__builtin_bit_cast(bf16x8_t, a), __builtin_bit_cast(bf16x8_t, b), c, 0, 0, 0);
}
DI unsigned pk2(float a, float b) {
    f32x2 v = {a, b};
    return __builtin_bit_cast(unsigned, __builtin_convertvector(v, bf16x2_t));
}
template <int S> DI u32x4 pack_step(const f32x16& x) {
    u32x4 p = {pk2(x[8 * S], x[8 * S + 1]), pk2(x[8 * S + 2], x[8 * S + 3]), pk2(x[8 * S + 4], x[8 * S + 5]), pk2(x[8 * S + 6], x[8 * S + 7])};
    return p;
}
DI u32x2 tr_rd(LAS char* p) {
    s16x4 v = __builtin_amdgcn_ds_read_tr16_b64_v4i16((LAS s16x4*)p);
    return __builtin_bit_cast(u32x2, v);
}
DI u32x4 tr_rd2(LAS char* p0, LAS char* p1) {
    u32x2 a = tr_rd(p0), b = tr_rd(p1);
    u32x4 r = {a[0], a[1], b[0], b[1]};
    return r;
}
template <int LI, int SI> DI void dma16(unsigned lb, unsigned voff, rsrc_t rs, unsigned sb) {
    unsigned tmp;
    asm volatile("s_add_u32 m0, %1, %5\n\ts_add_u32 %0, %4, %6\n\tbuffer_load_dwordx4 %2, %3, %0 offen lds"
                 : "=&s"(tmp) : "s"(lb), "v"(voff), "s"(rs), "s"(sb), "n"(LI), "n"(SI) : "scc");
}
template <int LI, int SI> DI void dma16m(unsigned lb, unsigned voff, rsrc_t rs, unsigned sb, unsigned long long mask) {
    unsigned tmp;
    asm volatile("s_add_u32 m0, %1, %5\n\ts_add_u32 %0, %4, %6\n\ts_mov_b64 exec, %7\n\tbuffer_load_dwordx4 %2, %3, %0 offen lds\n\ts_mov_b64 exec, -1"
                 : "=&s"(tmp) : "s"(lb), "v"(voff), "s"(rs), "s"(sb), "n"(LI), "n"(SI), "s"(mask) : "scc");
}
template <int N> DI void wait_vm_bar() { asm volatile("s_waitcnt vmcnt(%0) lgkmcnt(0)\n\ts_barrier" ::"n"(N) : "memory"); }
template <int N> DI void wait_vm() { asm volatile("s_waitcnt vmcnt(%0)" ::"n"(N) : "memory"); }
DI void bar_lds() { asm volatile("s_waitcnt lgkmcnt(0)\n\ts_barrier" ::: "memory"); }
DI float from_hi(float v) {
    auto r = __builtin_amdgcn_permlane32_swap(__float_as_uint(v), __float_as_uint(v), false, false);
    return __uint_as_float(r[1]);
}
DI float pair_max(float v) {
    auto r = __builtin_amdgcn_permlane32_swap(__float_as_uint(v), __float_as_uint(v), false, false);
    return fmaxf(__uint_as_float(r[0]), __uint_as_float(r[1]));
}
DI float pair_min(float v) {
    auto r = __builtin_amdgcn_permlane32_swap(__float_as_uint(v), __float_as_uint(v), false, false);
    return fminf(__uint_as_float(r[0]), __uint_as_float(r[1]));
}
DI float pair_sum(float v) {
    auto r = __builtin_amdgcn_permlane32_swap(__float_as_uint(v), __float_as_uint(v), false, false);
    return __uint_as_float(r[0]) + __uint_as_float(r[1]);
}
DI u32x4 ld_sc1(rsrc_t rs, unsigned voff, unsigned soff) { return __builtin_amdgcn_raw_buffer_load_b128(rs, voff, soff, 16); }
DI unsigned ld_sc1_32(rsrc_t rs, unsigned voff, unsigned soff) { return __builtin_amdgcn_raw_buffer_load_b32(rs, voff, soff, 16); }
DI void st_sc1(u32x4 v, rsrc_t rs, unsigned voff, unsigned soff) { __builtin_amdgcn_raw_buffer_store_b128(v, rs, voff, soff, 16); }
DI void st_sc1_64(u32x2 v, rsrc_t rs, unsigned voff, unsigned soff) { __builtin_amdgcn_raw_buffer_store_b64(v, rs, voff, soff, 16); }
DI void st_sc1_32(unsigned v, rsrc_t rs, unsigned voff, unsigned soff) { __builtin_amdgcn_raw_buffer_store_b32(v, rs, voff, soff, 16); }

static constexpr float LOG2E = 1.4426950408889634f;

DI void phi_map(const f32x16& p, bool hh, f32x16& ep, f32x16& en) {
    float mx = p[0], mn = p[0];
#pragma unroll
    for (int i = 1; i < 16; ++i) { mx = fmaxf(mx, p[i]); mn = fminf(mn, p[i]); }
    mx = pair_max(mx); mn = pair_min(mn);
    const float bp = -mx * LOG2E, bn = mn * LOG2E;
    const float bpp = hh ? -__builtin_inff() : bp, bnp = hh ? -__builtin_inff() : bn;
    float sp = 0.f, sn = 0.f;
#pragma unroll
    for (int i = 0; i < 16; ++i) {
        ep[i] = __builtin_amdgcn_exp2f(__builtin_fmaf(p[i], LOG2E, i >= 12 ? bpp : bp));
        en[i] = __builtin_amdgcn_exp2f(__builtin_fmaf(p[i], -LOG2E, i >= 12 ? bnp : bn));
        sp += ep[i]; sn += en[i];
    }
    sp = pair_sum(sp); sn = pair_sum(sn);
    const float ip = __builtin_amdgcn_rcpf(sp), in_ = __builtin_amdgcn_rcpf(sn);
#pragma unroll
    for (int i = 0; i < 16; ++i) { ep[i] *= ip; en[i] *= in_; }
}

#define STAMP(k) do {} while (0)

__global__ void __launch_bounds__(512, 2)
fused_kernel(const unsigned short* __restrict__ x, const unsigned short* __restrict__ wA, const unsigned short* __restrict__ woA,
             const unsigned short* __restrict__ wB, const unsigned short* __restrict__ wphi, const unsigned short* __restrict__ woB,
             const unsigned short* __restrict__ gw, unsigned short* __restrict__ out, unsigned char* __restrict__ ws) {
    __shared__ __attribute__((aligned(16))) char smem[LDS_BYTES];
    LAS char* lds = (LAS char*)smem;
    const unsigned lds0 = (unsigned)(uintptr_t)lds;
    const int lane = threadIdx.x & 63;
    const int w = __builtin_amdgcn_readfirstlane(threadIdx.x >> 6);
    const int b = blockIdx.x;
    const int xcd = b & 7, rr = b >> 3, tl = rr & 7, ul = rr >> 3;
    const int t = (xcd & 1) * 8 + tl, u = (xcd >> 1) * 4 + ul, br = u >> 3, hp = u & 7;
    const int hd = w & 1, tg = w >> 1;
    const int r = lane & 31, hh = lane >> 5;
    const int q4 = (lane >> 2) & 3, p4 = lane & 3, blk = (lane >> 4) & 1;
#define ROT 0
    const int rot = ROT;
    STAMP(0);

    const rsrc_t rsX = __builtin_amdgcn_make_buffer_rsrc((void*)x, 0, 2048 * 896, 0x00020000);
    const rsrc_t rsW = __builtin_amdgcn_make_buffer_rsrc((void*)(br ? wB : wA), 0, 448 * 2688, 0x00020000);
    const rsrc_t rsS = __builtin_amdgcn_make_buffer_rsrc((void*)ws, 0, WS_END, 0x00020000);
    const rsrc_t rsG = __builtin_amdgcn_make_buffer_rsrc((void*)gw, 0, 896, 0x00020000);

    const unsigned vx = (unsigned)((t * 128 + 8 * w + (lane >> 3)) * 896 + (((lane & 7) ^ ((lane >> 4) + 4 * (w & 1))) << 4));
    const int cw = (lane & 7) ^ (((lane >> 4) & 1) << 2);
    const unsigned vw = (unsigned)((8 * w + (lane >> 3)) * 2688 + hp * 112 + ((cw == 7 ? 6 : cw) << 4));
    const unsigned lw = lds0 + w * 1024;
    const unsigned long long qmask = ~0x0808808008088080ull;
    auto kc_of = [&](int s) { int k = s + rot; return k >= 7 ? k - 7 : k; };

    auto load_gg = [&](unsigned& gword, unsigned& gate16) {
        const unsigned zero = 0;
        const int gs = w < 7 ? w : 6;
        asm volatile("s_nop 4\n\tbuffer_load_dword %0, %2, %3, 0 offen sc1\n\tbuffer_load_ushort %1, %4, %5, %6 offen"
                     : "=&v"(gword), "=&v"(gate16)
                     : "v"(zero), "s"(rsS), "v"(lane * 2), "s"(rsG), "s"(kc_of(gs) * 128));
    };
    const int xrow = tg * 32 + r;
    unsigned xoff[4];
#pragma unroll
    for (int ks = 0; ks < 4; ++ks) xoff[ks] = (unsigned)(xrow * 128 + (((2 * ks + hh) ^ ((r >> 1) & 7)) << 4));
    const int wcol = hd * 56 + blk * 32 + p4 * 8;
    const int wcolq = (hd == 0 && blk == 1 && p4 == 3) ? 120 : wcol;
    const unsigned wkv = (unsigned)((8 * hh + q4) * 128 + (wcol ^ ((q4 >> 1) << 6)));
    const unsigned wq = (unsigned)((8 * hh + q4) * 128 + (wcolq ^ ((q4 >> 1) << 6)));
    const unsigned gpos = (unsigned)(lane * 128 + ((7 ^ (((lane >> 1) & 1) << 2)) << 4));

    const int ct = rr & 7, tg64 = xcd * 4 + (rr >> 3);
    const bool do_out = ct != 7;
    const rsrc_t rsOA = __builtin_amdgcn_make_buffer_rsrc((void*)woA, 0, 448 * 896, 0x00020000);
    const rsrc_t rsOB = __builtin_amdgcn_make_buffer_rsrc((void*)woB, 0, 448 * 896, 0x00020000);
    const unsigned vo = (unsigned)((8 * w + (lane >> 3)) * 896 + ct * 128 + (cw << 4));
    const unsigned szero = 0;
    auto dma_wo = [&](auto N_) {
        constexpr int n = decltype(N_)::value;
        if constexpr (n < 7) dma16<n * 8192, n * 64 * 896>(lw, vo, rsOA, szero);
        else dma16<n * 8192, (n - 7) * 64 * 896>(lw, vo, rsOB, szero);
    };

    f32x16 zero16;
#pragma unroll
    for (int i = 0; i < 16; ++i) zero16[i] = 0.f;
    unsigned tag;
    u32x4 of0, of1;

    if (br == 0) {
        const int hrow0 = t == 0 ? 0 : t * 128 - 16;
        const unsigned vh = (unsigned)((hrow0 + 2 * w + ((lane >> 3) & 1)) * 896 + (((lane & 7) ^ w) << 4));
        const unsigned lh = lds0 + w * 256;
        auto issue = [&](auto S_, auto N_) {
            constexpr int s = decltype(S_)::value, n = decltype(N_)::value;
            constexpr int sl = ((s + 2) % 3) * A_SLOT;
            const unsigned kx = (unsigned)kc_of(s) * 128u, kw = (unsigned)kc_of(s) * (64u * 2688u);
            if constexpr (n == 0) { dma16<sl, 0>(lw, vx, rsX, kx); dma16<sl + 8192, 64 * 896>(lw, vx, rsX, kx); }
            if constexpr (n == 1) { dma16m<sl + A_W, 0>(lw, vw, rsW, kw, qmask); dma16<sl + A_W + 8192, 896>(lw, vw, rsW, kw); }
            if constexpr (n == 2) { dma16<sl + A_W + 16384, 1792>(lw, vw, rsW, kw); dma16m<sl + A_HALO, 0>(lh, vh, rsX, kx, 0xffffull); }
        };
        unsigned gword, gate16;
        load_gg(gword, gate16);
        issue(ic<0>{}, ic<0>{}); issue(ic<0>{}, ic<1>{}); issue(ic<0>{}, ic<2>{});
        issue(ic<1>{}, ic<0>{}); issue(ic<1>{}, ic<1>{}); issue(ic<1>{}, ic<2>{});
        issue(ic<2>{}, ic<0>{}); issue(ic<2>{}, ic<1>{}); issue(ic<2>{}, ic<2>{});
        asm volatile("s_waitcnt vmcnt(18)" : "+v"(gword), "+v"(gate16));
        tag = __builtin_amdgcn_readfirstlane(gword) + 1u;
        const u32x4 gchunk = {gate16, 0u, gate16, 0u};
        if (w < 3) *(LAS u32x4*)(lds + ((w + 2) % 3) * A_SLOT + A_W + gpos) = gchunk;
        unsigned hoff[4];
#pragma unroll
        for (int ks = 0; ks < 4; ++ks) hoff[ks] = (unsigned)(A_HALO + (r & 15) * 128 + (((2 * ks + hh) ^ ((r >> 1) & 7)) << 4));

        f32x16 aq = zero16, ak = zero16, av = zero16, ah = zero16;
        auto stage = [&](auto S_, auto R_) {
            constexpr int S = decltype(S_)::value;
            constexpr int ROLE = decltype(R_)::value;
            constexpr int SL = ((S + 2) % 3) * A_SLOT;
            if constexpr (S == 0) wait_vm_bar<12>();
            else if constexpr (S < 6) wait_vm_bar<6>();
            else wait_vm_bar<0>();
            STAMP(1 + S);
            if constexpr (S >= 1 && S <= 4) {
                if (w == S + 2) *(LAS u32x4*)(lds + ((S + 4) % 3) * A_SLOT + A_W + gpos) = gchunk;
            }
            auto kstep = [&](auto K_) {
                constexpr int ks = decltype(K_)::value;
                const u32x4 xf = *(LAS u32x4*)(lds + SL + xoff[ks]);
                LAS char* wb = lds + SL + A_W + ks * 2048;
                const u32x4 fq = tr_rd2(wb + wq, wb + wq + 512);
                const u32x4 fk = tr_rd2(wb + 8192 + wkv, wb + 8192 + wkv + 512);
                const u32x4 fv = tr_rd2(wb + 16384 + wkv, wb + 16384 + wkv + 512);
                aq = mfma(fq, xf, aq);
                ak = mfma(fk, xf, ak);
                av = mfma(xf, fv, av);
                if constexpr (ROLE < 2) {
                    const u32x4 hf = *(LAS u32x4*)(lds + SL + hoff[ks]);
                    if constexpr (ROLE == 0) ah = mfma(fk, hf, ah);
                    else ah = mfma(hf, fv, ah);
                }
                __builtin_amdgcn_sched_barrier(0x186);
                if constexpr (S >= 1 && S <= 4 && ks < 3) issue(ic<S + 2>{}, K_);
                if constexpr (S == 6) {
                    if (do_out) {
                        if constexpr (ks < 3) { dma_wo(ic<3 * ks>{}); dma_wo(ic<3 * ks + 1>{}); dma_wo(ic<3 * ks + 2>{}); }
                        else dma_wo(ic<9>{});
                    }
                }
            };
            kstep(ic<0>{}); kstep(ic<1>{}); kstep(ic<2>{}); kstep(ic<3>{});
        };
        auto gemm = [&](auto R_) {
            stage(ic<0>{}, R_); stage(ic<1>{}, R_); stage(ic<2>{}, R_); stage(ic<3>{}, R_); stage(ic<4>{}, R_); stage(ic<5>{}, R_); stage(ic<6>{}, R_);
        };
        if (tg == 0) gemm(ic<0>{});
        else if (tg == 1) gemm(ic<1>{});
        else gemm(ic<2>{});
        STAMP(8);

        const float gl = from_hi(aq[12]);
        u32x4 qf0 = pack_step<0>(aq), qf1 = pack_step<1>(aq);
        if (hh) { qf1[2] = 0u; qf1[3] = 0u; }
        const u32x4 kf0 = pack_step<0>(ak), kf1 = pack_step<1>(ak);
        const u32x4 vf0 = pack_step<0>(av), vf1 = pack_step<1>(av);
        {
            LAS char* xb = lds + A_XCH + hd * 12288 + lane * 16;
            if (tg < 3) {
                *(LAS u32x4*)(xb + (tg + 1) * 3072) = kf0;
                *(LAS u32x4*)(xb + (tg + 1) * 3072 + 1024) = kf1;
                *(LAS u32x4*)(xb + (tg + 1) * 3072 + 2048) = vf1;
            }
            if (tg == 0) {
                *(LAS u32x4*)(xb) = pack_step<0>(ah);
                *(LAS u32x4*)(xb + 1024) = pack_step<1>(ah);
            } else if (tg == 1) {
                *(LAS u32x4*)(xb + 2048) = pack_step<1>(ah);
            }
        }
        f32x16 so = mfma(kf0, qf0, zero16);
        so = mfma(kf1, qf1, so);
        bar_lds();
        STAMP(10);
        if (do_out) { dma_wo(ic<10>{}); dma_wo(ic<11>{}); dma_wo(ic<12>{}); dma_wo(ic<13>{}); }
        u32x4 pk0, pk1, pv1;
        {
            LAS char* xb = lds + A_XCH + hd * 12288 + tg * 3072 + lane * 16;
            pk0 = *(LAS u32x4*)(xb); pk1 = *(LAS u32x4*)(xb + 1024); pv1 = *(LAS u32x4*)(xb + 2048);
        }
        f32x16 sp = mfma(pk0, qf0, zero16);
        sp = mfma(pk1, qf1, sp);
        const bool par = r & 1;
        unsigned pmask = par ? 0xffffffffu : 0u;
        asm("" : "+v"(pmask));
        const int base = (r & ~1) - 4 * hh;
        const bool noprev = (t == 0) && (tg == 0);
        float c[12];
        float mx = -__builtin_inff();
#pragma unroll
        for (int m = 0; m < 12; ++m) {
            const int mm = m < 8 ? m : m - 4;
            const int cm = 2 * (mm & 1) + 8 * (mm >> 1) - (m < 8 ? 0 : 32);
            const float ve = m < 8 ? so[2 * mm] : sp[2 * mm], vo_ = m < 8 ? so[2 * mm + 1] : sp[2 * mm + 1];
            const float v = __uint_as_float((__float_as_uint(vo_) & pmask) | (__float_as_uint(ve) & ~pmask));
            bool ok = (unsigned)(base - cm) <= 14u;
            if (m >= 8) ok = ok && !noprev;
            c[m] = ok ? v : -__builtin_inff();
            mx = fmaxf(mx, c[m]);
        }
        mx = pair_max(mx);
        const float SC = 0.18898223650461362f * LOG2E;
        const float bias = -mx * SC;
        float sum = 0.f;
        const unsigned pm = pmask ^ 0x0000ffffu;
        unsigned pw[12];
#pragma unroll
        for (int m = 0; m < 12; ++m) {
            const float e = __builtin_amdgcn_exp2f(__builtin_fmaf(c[m], SC, bias));
            sum += e;
            pw[m] = pk2(e, e) & pm;
        }
        sum = pair_sum(sum);
        const u32x4 pf0 = {pw[0], pw[1], pw[2], pw[3]}, pf1 = {pw[4], pw[5], pw[6], pw[7]}, pfp = {pw[8], pw[9], pw[10], pw[11]};
        f32x16 o = mfma(vf0, pf0, zero16);
        o = mfma(vf1, pf1, o);
        o = mfma(pv1, pfp, o);
        const float g = __builtin_amdgcn_rcpf(1.f + __builtin_amdgcn_exp2f(-gl * LOG2E));
        const float sc = g * __builtin_amdgcn_rcpf(sum);
#pragma unroll
        for (int i = 0; i < 16; ++i) o[i] *= sc;
        of0 = pack_step<0>(o); of1 = pack_step<1>(o);
        if (hh) { of1[2] = 0u; of1[3] = 0u; }
        STAMP(9);
    } else {
        const rsrc_t rsP = __builtin_amdgcn_make_buffer_rsrc((void*)wphi, 0, 16 * 1568, 0x00020000);
        if (w < 4) {
            const int c = w * 64 + lane;
            dma16<B_PHIW, 0>(lw, (unsigned)((c < 196 ? c : 195) * 16 + hp * 3136), rsP, szero);
        }
        auto issue = [&](auto S_, auto N_) {
            constexpr int s = decltype(S_)::value, n = decltype(N_)::value;
            constexpr int sl = (s % 3) * B_SLOT;
            const unsigned kx = (unsigned)kc_of(s) * 128u, kw = (unsigned)kc_of(s) * (64u * 2688u);
            if constexpr (n == 0) dma16<sl, 0>(lw, vx, rsX, kx);
            if constexpr (n == 1) dma16<sl + 8192, 64 * 896>(lw, vx, rsX, kx);
            if constexpr (n == 2) dma16<sl + B_W, 896>(lw, vw, rsW, kw);
            if constexpr (n == 3) dma16<sl + B_W + 8192, 1792>(lw, vw, rsW, kw);
        };
        auto issue_q = [&](auto S_) {
            constexpr int s = decltype(S_)::value;
            dma16m<B_WQ + s * 8192, 0>(lw, vw, rsW, (unsigned)kc_of(s) * (64u * 2688u), qmask);
        };
        unsigned gword, gate16;
        load_gg(gword, gate16);
        issue(ic<0>{}, ic<0>{}); issue(ic<0>{}, ic<1>{}); issue(ic<0>{}, ic<2>{}); issue(ic<0>{}, ic<3>{});
        issue(ic<1>{}, ic<0>{}); issue(ic<1>{}, ic<1>{}); issue(ic<1>{}, ic<2>{}); issue(ic<1>{}, ic<3>{});
        issue(ic<2>{}, ic<0>{}); issue(ic<2>{}, ic<1>{}); issue(ic<2>{}, ic<2>{}); issue(ic<2>{}, ic<3>{});
        asm volatile("s_waitcnt vmcnt(12)" : "+v"(gword), "+v"(gate16));
        tag = __builtin_amdgcn_readfirstlane(gword) + 1u;
        {
            const u32x4 gchunk = {gate16, 0u, gate16, 0u};
            if (w < 7) *(LAS u32x4*)(lds + B_WQ + w * 8192 + gpos) = gchunk;
        }
        u32x4 xf[28];
        f32x16 ak = zero16, av = zero16;
        u32x4 wp0, wp1;
        auto stage = [&](auto S_) {
            constexpr int S = decltype(S_)::value;
            constexpr int SL = (S % 3) * B_SLOT;
            if constexpr (S == 0) wait_vm_bar<8>();
            else wait_vm_bar<4>();
            STAMP(1 + S);
            if constexpr (S == 1) {
                const int fc = (blk == 1 && p4 == 3) ? 0 : (blk * 32 + p4 * 8);
                LAS char* pb = lds + B_PHIW + hd * 1568 + (4 * hh + q4) * 56 + fc;
                wp0 = tr_rd2(pb, pb + 8 * 56);
                wp1 = tr_rd2(pb + 16 * 56, hh ? pb : pb + 24 * 56);
                if (hh) { wp1[2] = 0u; wp1[3] = 0u; }
            }
            auto kstep = [&](auto K_) {
                constexpr int ks = decltype(K_)::value;
                xf[S * 4 + ks] = *(LAS u32x4*)(lds + SL + xoff[ks]);
                LAS char* wb = lds + SL + B_W + ks * 2048;
                const u32x4 fk = tr_rd2(wb + wkv, wb + wkv + 512);
                const u32x4 fv = tr_rd2(wb + 8192 + wkv, wb + 8192 + wkv + 512);
                ak = mfma(fk, xf[S * 4 + ks], ak);
                av = mfma(xf[S * 4 + ks], fv, av);
                __builtin_amdgcn_sched_barrier(0x186);
                if constexpr (S >= 1 && S <= 4) issue(ic<S + 2>{}, K_);
                if constexpr (S == 5) issue_q(K_);
                if constexpr (S == 6 && ks < 3) issue_q(ic<4 + ks>{});
            };
            kstep(ic<0>{}); kstep(ic<1>{}); kstep(ic<2>{}); kstep(ic<3>{});
        };
        stage(ic<0>{}); stage(ic<1>{}); stage(ic<2>{}); stage(ic<3>{}); stage(ic<4>{}); stage(ic<5>{}); stage(ic<6>{});
        STAMP(8);
        {
            f32x16 p = mfma(wp0, pack_step<0>(ak), zero16);
            p = mfma(wp1, pack_step<1>(ak), p);
            u32x4 vf0 = pack_step<0>(av), vf1 = pack_step<1>(av);
            if (r == 28) { vf0 = (u32x4){0x3F803F80u, 0x3F803F80u, 0x3F803F80u, 0x3F803F80u}; vf1 = vf0; }
            LAS char* vb = lds + B_VF + (hd * 4 + tg) * 2048 + lane * 16;
            *(LAS u32x4*)(vb) = vf0;
            *(LAS u32x4*)(vb + 1024) = vf1;
            f32x16 ep, en;
            phi_map(p, hh, ep, en);
            LAS char* pb = lds + B_PHI + hd * 16384 + xrow * 64;
#pragma unroll
            for (int g = 0; g < 4; ++g) {
                const int so = ((2 * g + hh) ^ ((r >> 1) & 7)) << 3;
                *(LAS u32x2*)(pb + so) = (u32x2){pk2(ep[4 * g], ep[4 * g + 1]), pk2(ep[4 * g + 2], ep[4 * g + 3])};
                *(LAS u32x2*)(pb + 8192 + so) = (u32x2){pk2(en[4 * g], en[4 * g + 1]), pk2(en[4 * g + 2], en[4 * g + 3])};
            }
        }
        bar_lds();
        STAMP(9);
        if (w < 4) {
            const int sign = w >> 1;
            f32x16 kv = zero16;
            LAS char* pb0 = lds + B_PHI + hd * 16384 + sign * 8192 + (4 * hh + q4) * 64 + (((4 * blk + p4) ^ (2 * hh + (q4 >> 1))) << 3);
            LAS char* pb1 = lds + B_PHI + hd * 16384 + sign * 8192 + (8 + 4 * hh + q4) * 64 + (((4 * blk + p4) ^ (4 + 2 * hh + (q4 >> 1))) << 3);
            LAS char* vb = lds + B_VF + hd * 8192 + lane * 16;
#pragma unroll
            for (int i = 0; i < 8; ++i) {
                const u32x4 a = tr_rd2(pb0 + i * 1024, pb1 + i * 1024);
                const u32x4 v = *(LAS u32x4*)(vb + i * 1024);
                kv = mfma(a, v, kv);
            }
            const unsigned sb = WS_L1 + (unsigned)(((hp * 16 + (hd * 2 + sign) * 4) * 16 + t) * 2048);
#pragma unroll
            for (int i = 0; i < 8; ++i) {
                const u32x4 gr = {__float_as_uint(kv[2 * i]), tag, __float_as_uint(kv[2 * i + 1]), tag};
                st_sc1(gr, rsS, (unsigned)(lane * 16 + (i & 1) * 1024), sb + (i >> 1) * 32768);
            }
            wait_vm_bar<8>();
        } else {
            wait_vm_bar<0>();
        }
        STAMP(10);
        f32x16 aq = zero16;
        const bool reducer = (w >> 1) == 2;
        const bool wo_now = do_out && !reducer;
        auto qstep = [&](auto I_) {
            constexpr int i2 = decltype(I_)::value;
#pragma unroll
            for (int i = 2 * i2; i < 2 * i2 + 2; ++i) {
                LAS char* wb = lds + B_WQ + i * 2048;
                const u32x4 fq = tr_rd2(wb + wq, wb + wq + 512);
                aq = mfma(fq, xf[i], aq);
            }
            if constexpr (i2 < 12) {
                __builtin_amdgcn_sched_barrier(0x186);
                if (wo_now) dma_wo(I_);
            }
        };
        qstep(ic<0>{}); qstep(ic<1>{}); qstep(ic<2>{}); qstep(ic<3>{}); qstep(ic<4>{}); qstep(ic<5>{}); qstep(ic<6>{});
        qstep(ic<7>{}); qstep(ic<8>{}); qstep(ic<9>{}); qstep(ic<10>{}); qstep(ic<11>{}); qstep(ic<12>{}); qstep(ic<13>{});
        STAMP(11);
        if (reducer) {
            const int half = w & 1;
            const unsigned sb = WS_L1 + (unsigned)((hp * 16 + t) * 32768 + half * 1024);
            float s0, s1;
            for (int spin = 0; spin < SPIN_CAP; ++spin) {
                asm volatile("" ::: "memory");
                u32x4 gr[16];
#pragma unroll
                for (int i = 0; i < 16; ++i) gr[i] = ld_sc1(rsS, (unsigned)(lane * 16), sb + i * 2048);
                bool ok = true;
                s0 = 0.f; s1 = 0.f;
#pragma unroll
                for (int i = 0; i < 16; ++i) {
                    ok = ok && gr[i][1] == tag && gr[i][3] == tag;
                    s0 += __uint_as_float(gr[i][0]); s1 += __uint_as_float(gr[i][2]);
                }
                if (__all(ok)) break;
                __builtin_amdgcn_s_sleep(1);
            }
            STAMP(16);
            st_sc1_64((u32x2){pk2(s0, s1), tag}, rsS, (unsigned)(lane * 16 + half * 8), WS_L2 + (unsigned)((hp * 16 + t) * 1024));
            if (do_out) {
                dma_wo(ic<0>{}); dma_wo(ic<1>{}); dma_wo(ic<2>{}); dma_wo(ic<3>{}); dma_wo(ic<4>{}); dma_wo(ic<5>{});
                dma_wo(ic<6>{}); dma_wo(ic<7>{}); dma_wo(ic<8>{}); dma_wo(ic<9>{}); dma_wo(ic<10>{}); dma_wo(ic<11>{});
            }
        }
        STAMP(12);
        const float gl = from_hi(aq[12]);
        u32x4 pq[4];
        {
            f32x16 p = mfma(wp0, pack_step<0>(aq), zero16);
            p = mfma(wp1, pack_step<1>(aq), p);
            f32x16 ep, en;
            phi_map(p, hh, ep, en);
            pq[0] = pack_step<0>(ep); pq[1] = pack_step<1>(ep); pq[2] = pack_step<0>(en); pq[3] = pack_step<1>(en);
        }
        const float g1 = __builtin_amdgcn_rcpf(1.f + __builtin_amdgcn_exp2f(gl * LOG2E));
        asm volatile("" :: "v"(pq[0]), "v"(pq[1]), "v"(pq[2]), "v"(pq[3]));
        STAMP(17);
        u32x4 kvf[4];
        {
            const unsigned sb = WS_L2 + (unsigned)((hp * 16 + w * 2) * 1024);
            u32x4 g0, g1;
            for (int spin = 0; spin < SPIN_CAP; ++spin) {
                asm volatile("" ::: "memory");
                g0 = ld_sc1(rsS, (unsigned)(lane * 16), sb);
                g1 = ld_sc1(rsS, (unsigned)(lane * 16), sb + 1024);
                const bool ok = g0[1] == tag && g0[3] == tag && g1[1] == tag && g1[3] == tag;
                if (__all(ok)) break;
                __builtin_amdgcn_s_sleep(L2SLEEP);
            }
            *(LAS u32x4*)(lds + B_PHIW + w * 1024 + lane * 16) = (u32x4){g0[0], g0[2], g1[0], g1[2]};
            bar_lds();
#pragma unroll
            for (int i = 0; i < 4; ++i) kvf[i] = *(LAS u32x4*)(lds + B_PHIW + (hd * 4 + i) * 1024 + lane * 16);
        }
        STAMP(13);
        f32x16 o = mfma(kvf[0], pq[0], zero16);
        o = mfma(kvf[1], pq[1], o);
        o = mfma(kvf[2], pq[2], o);
        o = mfma(kvf[3], pq[3], o);
        const float den = from_hi(o[12]) + 1e-6f;
        const float sc = g1 * __builtin_amdgcn_rcpf(den);
#pragma unroll
        for (int i = 0; i < 16; ++i) o[i] *= sc;
        of0 = pack_step<0>(o); of1 = pack_step<1>(o);
        if (hh) { of1[2] = 0u; of1[3] = 0u; }
    }

    {
        const unsigned sb = WS_OF + (unsigned)(((((t * 4 + tg) * 16 + u) * 2 + hd) * 2) * 1024);
        st_sc1(of0, rsS, (unsigned)(lane * 16), sb);
        st_sc1(of1, rsS, (unsigned)(lane * 16), sb + 1024);
        wait_vm_bar<0>();
        if (w == 0) {
            if (lane == 0) st_sc1_32(tag, rsS, 0u, WS_FLAG + (unsigned)((t * 16 + u) * 128));
        }
    }
    STAMP(14);
    if (do_out) {
        if (br == 1) { dma_wo(ic<12>{}); dma_wo(ic<13>{}); }
        const int tgl = w & 1, kq = w >> 1;
        const int ot = tg64 >> 1, otg = tg64 * 2 + tgl;
        const unsigned wob = (unsigned)((kq * 112 + 4 * hh + q4) * 128);
        const unsigned woc0 = (unsigned)((blk * 32 + p4 * 8) ^ ((q4 >> 1) << 6));
        const unsigned woc1 = woc0 ^ 64u;
        const unsigned pad = hh ? 20 * 128 : 24 * 128;
        f32x16 acc0 = zero16, acc1 = zero16;
        auto wait_flags = [&](int ub) {
            const unsigned fo = (unsigned)((ot * 16 + ub + 2 * kq + (lane & 1)) * 128);
            for (int spin = 0; spin < SPIN_CAP; ++spin) {
                asm volatile("" ::: "memory");
                const unsigned f = ld_sc1_32(rsS, fo, WS_FLAG);
                if (__all(f == tag)) break;
                __builtin_amdgcn_s_sleep(1);
            }
            asm volatile("" ::: "memory");
        };
        auto load_o = [&](int ub, u32x4 (&of)[8]) {
            const unsigned sb = WS_OF + (unsigned)((otg * 16 + ub + 2 * kq) * 4096);
#pragma unroll
            for (int i = 0; i < 8; ++i) of[i] = ld_sc1(rsS, (unsigned)(lane * 16), sb + i * 1024);
        };
        auto read_wo = [&](int rowb, u32x4 (&wf)[16]) {
#pragma unroll
            for (int i = 0; i < 8; ++i) {
                LAS char* p = lds + rowb * 128 + wob + ((i >> 1) * 28 + (i & 1) * 16) * 128;
                LAS char* p2 = (i & 1) ? lds + rowb * 128 + wob + (i >> 1) * 28 * 128 + pad : p + 1024;
                wf[2 * i] = tr_rd2(p + woc0, p2 + woc0);
                wf[2 * i + 1] = tr_rd2(p + woc1, p2 + woc1);
            }
        };
        u32x4 of[8], wf[16];
        read_wo(0, wf);
        wait_flags(0);
        STAMP(18);
        load_o(0, of);
#pragma unroll
        for (int i = 0; i < 8; ++i) { acc0 = mfma(wf[2 * i], of[i], acc0); acc1 = mfma(wf[2 * i + 1], of[i], acc1); }
        asm volatile("" :: "v"(acc0), "v"(acc1));
        STAMP(19);
        wait_vm_bar<0>();
        read_wo(448, wf);
        STAMP(15);
        wait_flags(8);
        STAMP(20);
        load_o(8, of);
#pragma unroll
        for (int i = 0; i < 8; ++i) { acc0 = mfma(wf[2 * i], of[i], acc0); acc1 = mfma(wf[2 * i + 1], of[i], acc1); }
        asm volatile("" :: "v"(acc0), "v"(acc1));
        STAMP(21);
        {
            LAS char* rb = lds + tgl * 24576 + lane * 16;
#pragma unroll
            for (int qd = 0; qd < 8; ++qd) {
                const int own = qd >> 1;
                const f32x16& a = qd < 4 ? acc0 : acc1;
                const int g = qd & 3;
                const f32x4 v = {a[4 * g], a[4 * g + 1], a[4 * g + 2], a[4 * g + 3]};
                if (own != kq) {
                    const int src = kq - (kq > own ? 1 : 0);
                    *(LAS f32x4*)(rb + own * 6144 + src * 2048 + (qd & 1) * 1024) = v;
                }
            }
            bar_lds();
            STAMP(22);
            f32x4 v0, v1;
            {
                const f32x16& a = kq < 2 ? acc0 : acc1;
                if (kq & 1) { v0 = (f32x4){a[8], a[9], a[10], a[11]}; v1 = (f32x4){a[12], a[13], a[14], a[15]}; }
                else { v0 = (f32x4){a[0], a[1], a[2], a[3]}; v1 = (f32x4){a[4], a[5], a[6], a[7]}; }
            }
#pragma unroll
            for (int s = 0; s < 3; ++s) {
                v0 += *(LAS f32x4*)(rb + kq * 6144 + s * 2048);
                v1 += *(LAS f32x4*)(rb + kq * 6144 + s * 2048 + 1024);
            }
            const unsigned a0 = pk2(v0[0], v0[1]), a1 = pk2(v0[2], v0[3]), b0 = pk2(v1[0], v1[1]), b1 = pk2(v1[2], v1[3]);
            auto s0 = __builtin_amdgcn_permlane32_swap(a0, b0, false, false);
            auto s1 = __builtin_amdgcn_permlane32_swap(a1, b1, false, false);
            const u32x4 ov = {s0[0], s1[0], s0[1], s1[1]};
            *(u32x4*)(out + (size_t)(otg * 32 + r) * 448 + ct * 64 + kq * 16 + hh * 8) = ov;
        }
    } else if (b == 56 && w == 0) {
        for (int spin = 0; spin < SPIN_CAP; ++spin) {
            asm volatile("" ::: "memory");
            bool ok = true;
#pragma unroll
            for (int i = 0; i < 4; ++i) ok = ok && ld_sc1_32(rsS, (unsigned)(lane * 128), WS_FLAG + i * 8192) == tag;
            if (__all(ok)) break;
            __builtin_amdgcn_s_sleep(2);
        }
        if (lane == 0) st_sc1_32(tag, rsS, 0u, WS_G);
    }
}

extern "C" void kernel_launch(void* const* d_in, const int* in_sizes, int n_in, void* d_out, int out_size, void* d_ws, size_t ws_size,
                              hipStream_t stream) {
    fused_kernel<<<256, 512, 0, stream>>>((const unsigned short*)d_in[0], (const unsigned short*)d_in[1], (const unsigned short*)d_in[2],
                                          (const unsigned short*)d_in[4], (const unsigned short*)d_in[5], (const unsigned short*)d_in[6],
                                          (const unsigned short*)d_in[7], (unsigned short*)d_out, (unsigned char*)d_ws);
}
```

```cpp
#include <hip/hip_runtime.h>
#include <stdint.h>

typedef __bf16 bf16x8_t __attribute__((ext_vector_type(8)));
typedef __bf16 bf16x2_t __attribute__((ext_vector_type(2)));
typedef short s16x4 __attribute__((ext_vector_type(4)));
typedef float f32x2 __attribute__((ext_vector_type(2)));
typedef float f32x4 __attribute__((ext_vector_type(4)));
typedef float f32x16 __attribute__((ext_vector_type(16)));
typedef unsigned u32x2 __attribute__((ext_vector_type(2)));
typedef unsigned u32x4 __attribute__((ext_vector_type(4)));
#define LAS __attribute__((address_space(3)))
#define DI __device__ __forceinline__
typedef __amdgpu_buffer_rsrc_t rsrc_t;

template <int N> struct ic { static constexpr int value = N; };
template <int B, int E, class F> DI void sfor(F&& f) {
    if constexpr (B < E) { f(ic<B>{}); sfor<B + 1, E>(f); }
}

static constexpr unsigned WS_G = 0;
static constexpr unsigned WS_START = 1024;
static constexpr unsigned WS_FR = 65536;
static constexpr unsigned WS_FO = 131072;
static constexpr unsigned WS_L2 = 262144;
static constexpr unsigned WS_L1 = 1u << 20;
static constexpr unsigned WS_OF = 5u << 20;
static constexpr unsigned WS_END = 9u << 20;

static constexpr int A_SLOT = 43008, A_HALO = 16384, A_W = 18432, A_XCH = 129024;
static constexpr int B_SLOT = 32768, B_WK = 16384, B_WV = 24576, B_WQ = 98304, B_PHIW = 155648, B_FLG = 159744;
static constexpr int B_PHI = 32768, B_VF = 65536, B_WO = 0;
static constexpr int G_TAG = B_FLG + 32, G_STASH = B_FLG + 256;
static constexpr int LDS_BYTES = 163840;
static constexpr int SPIN_CAP = 1 << 18;
static constexpr int H_WO = 5, H_WF = 6;
#define NCW 8
#define HPRIO 0
#define CWI 16
#define RDM 1
#define PSL 0
#define KPRIO 0
#define CODE_BYTES 46848
#define QPD 6
#define Q5 6
#define Q6 2
#define FRK 0
#define CLR_I 7
#define STG1 1
#define STG2 1
DI f32x16 mfma(u32x4 a, u32x4 b, f32x16 c) {
    return __builtin_amdgcn_mfma_f32_32x32x16_bf16(__builtin_bit_cast(bf16x8_t, a), __builtin_bit_cast(bf16x8_t, b), c, 0, 0, 0);
}
DI f32x4 mfma16(u32x4 a, u32x4 b, f32x4 c) {
    return __builtin_amdgcn_mfma_f32_16x16x32_bf16(__builtin_bit_cast(bf16x8_t, a), __builtin_bit_cast(bf16x8_t, b), c, 0, 0, 0);
}
DI unsigned pk2(float a, float b) {
    f32x2 v = {a, b};
    return __builtin_bit_cast(unsigned, __builtin_convertvector(v, bf16x2_t));
}
DI unsigned pk2v(f32x2 v) { return __builtin_bit_cast(unsigned, __builtin_convertvector(v, bf16x2_t)); }
template <int S> DI u32x4 pack_step(const f32x16& x) {
    u32x4 p = {pk2(x[8 * S], x[8 * S + 1]), pk2(x[8 * S + 2], x[8 * S + 3]), pk2(x[8 * S + 4], x[8 * S + 5]), pk2(x[8 * S + 6], x[8 * S + 7])};
    return p;
}
template <class T> DI void pin(T& v) { asm volatile("" : "+v"(v)); }
DI u32x2 tr_rd(LAS char* p) {
    s16x4 v = __builtin_amdgcn_ds_read_tr16_b64_v4i16((LAS s16x4*)p);
    return __builtin_bit_cast(u32x2, v);
}
DI u32x4 tr_rd2(LAS char* p0, LAS char* p1) {
    u32x2 a = tr_rd(p0), b = tr_rd(p1);
    u32x4 r = {a[0], a[1], b[0], b[1]};
    return r;
}
template <int LI, int SI> DI void dma1(unsigned lb, unsigned voff, rsrc_t rs, unsigned sb) {
    unsigned tmp;
    asm volatile("s_add_u32 m0, %1, %5\n\ts_add_u32 %0, %4, %6\n\tbuffer_load_dwordx4 %2, %3, %0 offen lds"
                 : "=&s"(tmp) : "s"(lb), "v"(voff), "s"(rs), "s"(sb), "n"(LI), "n"(SI) : "scc");
}
template <int LI, int SI> DI void dma1m(unsigned lb, unsigned voff, rsrc_t rs, unsigned sb, unsigned long long mask) {
    unsigned tmp;
    asm volatile("s_add_u32 m0, %1, %5\n\ts_add_u32 %0, %4, %6\n\ts_mov_b64 exec, %7\n\tbuffer_load_dwordx4 %2, %3, %0 offen lds\n\ts_mov_b64 exec, -1"
                 : "=&s"(tmp) : "s"(lb), "v"(voff), "s"(rs), "s"(sb), "n"(LI), "n"(SI), "s"(mask) : "scc");
}
template <int LI, int SI, int DS> DI void dma2(unsigned lb, unsigned v0, unsigned v1, rsrc_t rs, unsigned sb) {
    unsigned tmp;
    asm volatile("s_add_u32 m0, %1, %6\n\ts_add_u32 %0, %5, %7\n\tbuffer_load_dwordx4 %2, %4, %0 offen lds\n\t"
                 "s_add_u32 m0, %1, %8\n\ts_add_u32 %0, %5, %9\n\tbuffer_load_dwordx4 %3, %4, %0 offen lds"
                 : "=&s"(tmp) : "s"(lb), "v"(v0), "v"(v1), "s"(rs), "s"(sb), "n"(LI), "n"(SI), "n"(LI + 1024), "n"(SI + DS) : "scc");
}
template <int LI, int SI, int DS> DI void dma2m(unsigned lb, unsigned v0, rsrc_t rs, unsigned sb, unsigned long long mask) {
    unsigned tmp;
    asm volatile("s_add_u32 m0, %1, %5\n\ts_add_u32 %0, %4, %6\n\ts_mov_b64 exec, %9\n\tbuffer_load_dwordx4 %2, %3, %0 offen lds\n\t"
                 "s_add_u32 m0, %1, %7\n\ts_add_u32 %0, %4, %8\n\tbuffer_load_dwordx4 %2, %3, %0 offen lds\n\ts_mov_b64 exec, -1"
                 : "=&s"(tmp) : "s"(lb), "v"(v0), "s"(rs), "s"(sb), "n"(LI), "n"(SI), "n"(LI + 1024), "n"(SI + DS), "s"(mask) : "scc");
}
template <int N> DI void wait_vm_bar() { asm volatile("s_waitcnt vmcnt(%0) lgkmcnt(0)\n\ts_barrier" ::"n"(N) : "memory"); }
template <int N> DI void wait_vm() { asm volatile("s_waitcnt vmcnt(%0)" ::"n"(N) : "memory"); }
DI void bar_lds() { asm volatile("s_waitcnt lgkmcnt(0)\n\ts_barrier" ::: "memory"); }
DI void wait_lds() { asm volatile("s_waitcnt lgkmcnt(0)" ::: "memory"); }
DI void fence_sched() { __builtin_amdgcn_sched_barrier(0); }
DI float from_hi(float v) {
    auto r = __builtin_amdgcn_permlane32_swap(__float_as_uint(v), __float_as_uint(v), false, false);
    return __uint_as_float(r[1]);
}
DI float fmx(float a, float b) { return __builtin_elementwise_maximum(a, b); }
DI float fmn(float a, float b) { return __builtin_elementwise_minimum(a, b); }
DI float pair_max(float v) {
    auto r = __builtin_amdgcn_permlane32_swap(__float_as_uint(v), __float_as_uint(v), false, false);
    return fmx(__uint_as_float(r[0]), __uint_as_float(r[1]));
}
DI float pair_min(float v) {
    auto r = __builtin_amdgcn_permlane32_swap(__float_as_uint(v), __float_as_uint(v), false, false);
    return fmn(__uint_as_float(r[0]), __uint_as_float(r[1]));
}
DI float pair_sum(float v) {
    auto r = __builtin_amdgcn_permlane32_swap(__float_as_uint(v), __float_as_uint(v), false, false);
    return __uint_as_float(r[0]) + __uint_as_float(r[1]);
}
DI u32x4 ld_sc1(rsrc_t rs, unsigned voff, unsigned soff) { return __builtin_amdgcn_raw_buffer_load_b128(rs, voff, soff, 16); }
DI unsigned ld_sc1_32(rsrc_t rs, unsigned voff, unsigned soff) { return __builtin_amdgcn_raw_buffer_load_b32(rs, voff, soff, 16); }
DI void st_sc1(u32x4 v, rsrc_t rs, unsigned voff, unsigned soff) {
    __builtin_amdgcn_raw_buffer_store_b128(v, rs, voff, soff, 16);
    asm volatile("s_nop 1" ::"v"(v) : "memory");
}
DI void st_sc1_64(u32x2 v, rsrc_t rs, unsigned voff, unsigned soff) { __builtin_amdgcn_raw_buffer_store_b64(v, rs, voff, soff, 16); }
DI void st_sc1_32(unsigned v, rsrc_t rs, unsigned voff, unsigned soff) { __builtin_amdgcn_raw_buffer_store_b32(v, rs, voff, soff, 16); }

static constexpr unsigned EMPTY = 0xffffffffu;
DI unsigned umin(unsigned a, unsigned b) { return a < b ? a : b; }
DI unsigned umax(unsigned a, unsigned b) { return a > b ? a : b; }
DI u32x4 payload(u32x4 v) { return (u32x4){umin(v[0], EMPTY - 1u), umin(v[1], EMPTY - 1u), umin(v[2], EMPTY - 1u), umin(v[3], EMPTY - 1u)}; }
DI unsigned umax4(unsigned m, const u32x4& v) { return umax(umax(umax(m, v[0]), v[1]), umax(v[2], v[3])); }
DI unsigned touch(const void* p) {
    unsigned v;
    asm volatile("global_load_dword %0, %1, off" : "=v"(v) : "v"(p) : "memory");
    return v;
}
template <int N> DI void wait_flags(rsrc_t rs, unsigned voff, unsigned sb, unsigned tag) {
    unsigned f, cnt;
    asm volatile("s_mov_b32 %[c], 0x40000\n\t"
                 "s_mov_b64 exec, %[m]\n\t"
                 "1:\n\t"
                 "buffer_load_dword %[f], %[vo], %[rs], %[sb] offen sc1\n\t"
                 "s_waitcnt vmcnt(0)\n\t"
                 "v_cmpx_ne_u32 exec, %[tg], %[f]\n\t"
                 "s_cbranch_execz 2f\n\t"
                 "s_sleep 1\n\t"
                 "s_sub_u32 %[c], %[c], 1\n\t"
                 "s_cbranch_scc0 1b\n\t"
                 "2:\n\t"
                 "s_mov_b64 exec, -1"
                 : [f] "=&v"(f), [c] "=&s"(cnt)
                 : [vo] "v"(voff), [rs] "s"(rs), [sb] "s"(sb), [tg] "s"(tag), [m] "n"(N == 64 ? -1 : (1 << (N & 31)) - 1)
                 : "scc", "memory");
}
typedef unsigned u32x16 __attribute__((ext_vector_type(16)));
template <int I> DI u32x4 quad(const u32x16& v) { return (u32x4){v[4 * I], v[4 * I + 1], v[4 * I + 2], v[4 * I + 3]}; }
template <int ST> DI void fetch4(u32x16 (&g)[1], rsrc_t rs, unsigned voff, unsigned sb) {
    unsigned m0, m1, cnt;
    unsigned long long k[4];
    asm volatile(
                 "s_mov_b32 %[c], 0x10000\n\t"
                 "buffer_load_dwordx4 v[64:67], %[vo], %[rs], %[s0] offen offset:0 sc1\n\t"
                 "buffer_load_dwordx4 v[68:71], %[vo], %[rs], %[s0] offen offset:1024 sc1\n\t"
                 "buffer_load_dwordx4 v[72:75], %[vo], %[rs], %[s0] offen offset:2048 sc1\n\t"
                 "buffer_load_dwordx4 v[76:79], %[vo], %[rs], %[s0] offen offset:3072 sc1\n\t"
                 "1:\n\t"
                 "s_waitcnt vmcnt(0)\n\t"
                 "v_max3_u32 %[m0], v64, v65, v66\n\t"
                 "v_max_u32 %[m0], %[m0], v67\n\t"
                 "v_cmp_eq_u32 %[k0], -1, %[m0]\n\t"
                 "v_max3_u32 %[m1], v68, v69, v70\n\t"
                 "v_max_u32 %[m1], %[m1], v71\n\t"
                 "v_cmp_eq_u32 %[k1], -1, %[m1]\n\t"
                 "v_max3_u32 %[m0], v72, v73, v74\n\t"
                 "v_max_u32 %[m0], %[m0], v75\n\t"
                 "v_cmp_eq_u32 %[k2], -1, %[m0]\n\t"
                 "v_max3_u32 %[m1], v76, v77, v78\n\t"
                 "v_max_u32 %[m1], %[m1], v79\n\t"
                 "v_cmp_eq_u32 %[k3], -1, %[m1]\n\t"
                 "s_nop 1\n\t"
                 "s_or_b64 vcc, %[k0], %[k1]\n\t"
                 "s_or_b64 vcc, vcc, %[k2]\n\t"
                 "s_or_b64 vcc, vcc, %[k3]\n\t"
                 "s_cbranch_scc0 2f\n\t"
                 "s_sleep %[st]\n\t"
                 "s_mov_b64 exec, %[k0]\n\t"
                 "buffer_load_dwordx4 v[64:67], %[vo], %[rs], %[s0] offen offset:0 sc1\n\t"
                 "s_mov_b64 exec, %[k1]\n\t"
                 "buffer_load_dwordx4 v[68:71], %[vo], %[rs], %[s0] offen offset:1024 sc1\n\t"
                 "s_mov_b64 exec, %[k2]\n\t"
                 "buffer_load_dwordx4 v[72:75], %[vo], %[rs], %[s0] offen offset:2048 sc1\n\t"
                 "s_mov_b64 exec, %[k3]\n\t"
                 "buffer_load_dwordx4 v[76:79], %[vo], %[rs], %[s0] offen offset:3072 sc1\n\t"
                 "s_mov_b64 exec, -1\n\t"
                 "s_sub_u32 %[c], %[c], 1\n\t"
                 "s_cbranch_scc0 1b\n\t"
                 "s_waitcnt vmcnt(0)\n\t"
                 "2:\n\t"
                 : "=&{v[64:79]}"(g[0]), [m0] "=&v"(m0), [m1] "=&v"(m1), [c] "=&s"(cnt), [k0] "=&s"(k[0]), [k1] "=&s"(k[1]), [k2] "=&s"(k[2]), [k3] "=&s"(k[3])
                 : [vo] "v"(voff), [rs] "s"(rs), [s0] "s"(sb + 0u), [st] "n"(ST)
                 : "scc", "vcc", "memory");
}
template <int ST> DI void fetch8(u32x16 (&g)[2], rsrc_t rs, unsigned voff, unsigned sb) {
    unsigned m0, m1, cnt;
    unsigned long long k[8];
    asm volatile(
                 "s_mov_b32 %[c], 0x10000\n\t"
                 "buffer_load_dwordx4 v[64:67], %[vo], %[rs], %[s0] offen offset:0 sc1\n\t"
                 "buffer_load_dwordx4 v[68:71], %[vo], %[rs], %[s0] offen offset:1024 sc1\n\t"
                 "buffer_load_dwordx4 v[72:75], %[vo], %[rs], %[s0] offen offset:2048 sc1\n\t"
                 "buffer_load_dwordx4 v[76:79], %[vo], %[rs], %[s0] offen offset:3072 sc1\n\t"
                 "buffer_load_dwordx4 v[80:83], %[vo], %[rs], %[s1] offen offset:0 sc1\n\t"
                 "buffer_load_dwordx4 v[84:87], %[vo], %[rs], %[s1] offen offset:1024 sc1\n\t"
                 "buffer_load_dwordx4 v[88:91], %[vo], %[rs], %[s1] offen offset:2048 sc1\n\t"
                 "buffer_load_dwordx4 v[92:95], %[vo], %[rs], %[s1] offen offset:3072 sc1\n\t"
                 "1:\n\t"
                 "s_waitcnt vmcnt(0)\n\t"
                 "v_max3_u32 %[m0], v64, v65, v66\n\t"
                 "v_max_u32 %[m0], %[m0], v67\n\t"
                 "v_cmp_eq_u32 %[k0], -1, %[m0]\n\t"
                 "v_max3_u32 %[m1], v68, v69, v70\n\t"
                 "v_max_u32 %[m1], %[m1], v71\n\t"
                 "v_cmp_eq_u32 %[k1], -1, %[m1]\n\t"
                 "v_max3_u32 %[m0], v72, v73, v74\n\t"
                 "v_max_u32 %[m0], %[m0], v75\n\t"
                 "v_cmp_eq_u32 %[k2], -1, %[m0]\n\t"
                 "v_max3_u32 %[m1], v76, v77, v78\n\t"
                 "v_max_u32 %[m1], %[m1], v79\n\t"
                 "v_cmp_eq_u32 %[k3], -1, %[m1]\n\t"
                 "v_max3_u32 %[m0], v80, v81, v82\n\t"
                 "v_max_u32 %[m0], %[m0], v83\n\t"
                 "v_cmp_eq_u32 %[k4], -1, %[m0]\n\t"
                 "v_max3_u32 %[m1], v84, v85, v86\n\t"
                 "v_max_u32 %[m1], %[m1], v87\n\t"
                 "v_cmp_eq_u32 %[k5], -1, %[m1]\n\t"
                 "v_max3_u32 %[m0], v88, v89, v90\n\t"
                 "v_max_u32 %[m0], %[m0], v91\n\t"
                 "v_cmp_eq_u32 %[k6], -1, %[m0]\n\t"
                 "v_max3_u32 %[m1], v92, v93, v94\n\t"
                 "v_max_u32 %[m1], %[m1], v95\n\t"
                 "v_cmp_eq_u32 %[k7], -1, %[m1]\n\t"
                 "s_nop 1\n\t"
                 "s_or_b64 vcc, %[k0], %[k1]\n\t"
                 "s_or_b64 vcc, vcc, %[k2]\n\t"
                 "s_or_b64 vcc, vcc, %[k3]\n\t"
                 "s_or_b64 vcc, vcc, %[k4]\n\t"
                 "s_or_b64 vcc, vcc, %[k5]\n\t"
                 "s_or_b64 vcc, vcc, %[k6]\n\t"
                 "s_or_b64 vcc, vcc, %[k7]\n\t"
                 "s_cbranch_scc0 2f\n\t"
                 "s_sleep %[st]\n\t"
                 "s_mov_b64 exec, %[k0]\n\t"
                 "buffer_load_dwordx4 v[64:67], %[vo], %[rs], %[s0] offen offset:0 sc1\n\t"
                 "s_mov_b64 exec, %[k1]\n\t"
                 "buffer_load_dwordx4 v[68:71], %[vo], %[rs], %[s0] offen offset:1024 sc1\n\t"
                 "s_mov_b64 exec, %[k2]\n\t"
                 "buffer_load_dwordx4 v[72:75], %[vo], %[rs], %[s0] offen offset:2048 sc1\n\t"
                 "s_mov_b64 exec, %[k3]\n\t"
                 "buffer_load_dwordx4 v[76:79], %[vo], %[rs], %[s0] offen offset:3072 sc1\n\t"
                 "s_mov_b64 exec, %[k4]\n\t"
                 "buffer_load_dwordx4 v[80:83], %[vo], %[rs], %[s1] offen offset:0 sc1\n\t"
                 "s_mov_b64 exec, %[k5]\n\t"
                 "buffer_load_dwordx4 v[84:87], %[vo], %[rs], %[s1] offen offset:1024 sc1\n\t"
                 "s_mov_b64 exec, %[k6]\n\t"
                 "buffer_load_dwordx4 v[88:91], %[vo], %[rs], %[s1] offen offset:2048 sc1\n\t"
                 "s_mov_b64 exec, %[k7]\n\t"
                 "buffer_load_dwordx4 v[92:95], %[vo], %[rs], %[s1] offen offset:3072 sc1\n\t"
                 "s_mov_b64 exec, -1\n\t"
                 "s_sub_u32 %[c], %[c], 1\n\t"
                 "s_cbranch_scc0 1b\n\t"
                 "s_waitcnt vmcnt(0)\n\t"
                 "2:\n\t"
                 : "=&{v[64:79]}"(g[0]), "=&{v[80:95]}"(g[1]), [m0] "=&v"(m0), [m1] "=&v"(m1), [c] "=&s"(cnt), [k0] "=&s"(k[0]), [k1] "=&s"(k[1]), [k2] "=&s"(k[2]), [k3] "=&s"(k[3]), [k4] "=&s"(k[4]), [k5] "=&s"(k[5]), [k6] "=&s"(k[6]), [k7] "=&s"(k[7])
                 : [vo] "v"(voff), [rs] "s"(rs), [s0] "s"(sb + 0u), [s1] "s"(sb + 4096u), [st] "n"(ST)
                 : "scc", "vcc", "memory");
}

DI u32x4 flg_rd(LAS char* p) { return *(volatile LAS u32x4*)p; }
DI unsigned flg_min(u32x4 f) {
    const unsigned a = f[0] < f[1] ? f[0] : f[1], b = f[2] < f[3] ? f[2] : f[3];
    return __builtin_amdgcn_readfirstlane(a < b ? a : b);
}
DI void flg_wait(LAS char* p, unsigned need, u32x4 f) {
    if (__builtin_expect(flg_min(f) < need, 0)) {
        for (int spin = 0; spin < SPIN_CAP; ++spin) {
            __builtin_amdgcn_s_sleep(1);
            if (flg_min(flg_rd(p)) >= need) break;
        }
    }
    asm volatile("" ::: "memory");
}
template <int O = 0> DI void cnt_add(unsigned addr, unsigned one) {
    asm volatile("s_mov_b64 exec, 1\n\tds_add_u32 %0, %1 offset:%2\n\ts_mov_b64 exec, -1" :: "v"(addr), "v"(one), "n"(O) : "memory");
}
DI unsigned cnt_rd(LAS char* p) { return *(volatile LAS unsigned*)p; }
template <int N> DI void cnt_wait(LAS char* p, unsigned c) {
    if (__builtin_expect(__builtin_amdgcn_ballot_w64(c < N) != 0ull, 0)) {
        for (int spin = 0; spin < SPIN_CAP; ++spin) {
            if (__builtin_amdgcn_ballot_w64(cnt_rd(p) < N) == 0ull) break;
            __builtin_amdgcn_s_sleep(1);
        }
    }
    asm volatile("" ::: "memory");
}
DI void flg_set(LAS char* p, unsigned v) {
    asm volatile("" ::: "memory");
    *(volatile LAS unsigned*)p = v;
}

static constexpr float LOG2E = 1.4426950408889634f;

template <class HK> DI void phi_map(const f32x16& p, bool hh, unsigned (&fp)[8], unsigned (&fn)[8], HK&& hook) {
    float mx = p[0], mn = p[0];
#pragma unroll
    for (int i = 1; i < 16; ++i) { mx = fmx(mx, p[i]); mn = fmn(mn, p[i]); }
    mx = pair_max(mx); mn = pair_min(mn);
    const float bp = -mx * LOG2E, bn = mn * LOG2E;
    const float bpp = hh ? -__builtin_inff() : bp, bnp = hh ? -__builtin_inff() : bn;
    const f32x2 L = {LOG2E, LOG2E}, NL = {-LOG2E, -LOG2E};
    f32x2 ep[8], en[8];
    sfor<0, 8>([&](auto I_) {
        constexpr int i = decltype(I_)::value;
        const f32x2 v = {p[2 * i], p[2 * i + 1]};
        const float b0 = i >= 6 ? bpp : bp, b1 = i >= 6 ? bnp : bn;
        const f32x2 a = __builtin_elementwise_fma(v, L, (f32x2){b0, b0});
        const f32x2 c = __builtin_elementwise_fma(v, NL, (f32x2){b1, b1});
        hook(I_);
        ep[i] = (f32x2){__builtin_amdgcn_exp2f(a[0]), __builtin_amdgcn_exp2f(a[1])};
        en[i] = (f32x2){__builtin_amdgcn_exp2f(c[0]), __builtin_amdgcn_exp2f(c[1])};
    });
    f32x2 sp = ep[0], sn = en[0];
#pragma unroll
    for (int i = 1; i < 8; ++i) { sp += ep[i]; sn += en[i]; }
    const float ip = __builtin_amdgcn_rcpf(pair_sum(sp[0] + sp[1])), in_ = __builtin_amdgcn_rcpf(pair_sum(sn[0] + sn[1]));
#pragma unroll
    for (int i = 0; i < 8; ++i) { fp[i] = pk2v(ep[i] * (f32x2){ip, ip}); fn[i] = pk2v(en[i] * (f32x2){in_, in_}); }
}
DI void scale_pack(const f32x16& o, float sc, bool hh, u32x4& f0, u32x4& f1) {
    const f32x2 s2 = {sc, sc};
    unsigned q[8];
#pragma unroll
    for (int i = 0; i < 8; ++i) q[i] = pk2v((f32x2){o[2 * i], o[2 * i + 1]} * s2);
    f0 = (u32x4){q[0], q[1], q[2], q[3]};
    f1 = (u32x4){q[4], q[5], hh ? 0u : q[6], q[7]};
}

__global__ void __launch_bounds__(512, 2)
fused_kernel(const unsigned short* __restrict__ x, const unsigned short* __restrict__ wA, const unsigned short* __restrict__ woA,
             const unsigned short* __restrict__ wB, const unsigned short* __restrict__ wphi, const unsigned short* __restrict__ woB,
             const unsigned short* __restrict__ gw, unsigned short* __restrict__ out, unsigned char* ws) {
    __shared__ __attribute__((aligned(16))) char smem[LDS_BYTES];
    unsigned cpf;
    {
        unsigned long long pc;
        unsigned so;
        asm volatile("s_getpc_b64 %1\n\ts_and_b32 %2, %3, 0x38\n\ts_lshl_b32 %2, %2, 10\n\tv_lshl_add_u32 %0, %4, 4, %2\n\tv_min_u32 %0, %5, %0\n\tglobal_load_dword %0, %0, %1"
                     : "=&v"(cpf), "=&s"(pc), "=&s"(so) : "s"(blockIdx.x), "v"(threadIdx.x), "n"(CODE_BYTES) : "scc", "memory");
    }
    LAS char* lds = (LAS char*)smem;
    const unsigned lds0 = (unsigned)(uintptr_t)lds;
    int lq = threadIdx.x & 63, wq = __builtin_amdgcn_readfirstlane(threadIdx.x >> 6), bq = blockIdx.x;
    unsigned gword, gate0, gate1;
    if (wq >= 4) {
        const int lane = lq, j = wq & 3, xcd = bq & 7, tl = (bq >> 3) & 7, ul = (bq >> 6) & 3, rs7 = tl + ul + xcd;
        const unsigned rot = (unsigned)(rs7 - 7 * ((rs7 * 37) >> 8));
        const bool br = xcd < 4;
        const rsrc_t rsX = __builtin_amdgcn_make_buffer_rsrc((void*)x, 0, 2048 * 896, 0x00020000);
        const unsigned vxr = (unsigned)((((xcd & 1) * 8 + tl) * 128 + 32 * j + (lane >> 3)) * 896);
        const unsigned vx0 = vxr + (((lane & 7) ^ (lane >> 4)) << 4), vx1 = vxr + (((lane & 7) ^ ((lane >> 4) + 4)) << 4);
        const unsigned lxe = lds0 + j * 4096 + (br ? 0 : 2 * A_SLOT), kx = rot * 128u;
        dma2<0, 0, 8 * 896>(lxe, vx0, vx1, rsX, kx);
        dma2<2048, 16 * 896, 8 * 896>(lxe, vx0, vx1, rsX, kx);
        fence_sched();
        const rsrc_t rsW = __builtin_amdgcn_make_buffer_rsrc((void*)(br ? wB : wA), 0, 448 * 2688, 0x00020000);
        const int cw = (lane & 7) ^ (((lane >> 4) & 1) << 2);
        const unsigned vw = (unsigned)((16 * j + (lane >> 3)) * 2688 + (((xcd >> 1) & 1) * 4 + ul) * 112 + ((cw == 7 ? 6 : cw) << 4));
        const unsigned long long wmask = ~0x0808808008088080ull;
        const unsigned lwe = lds0 + j * 2048 + (br ? B_WK : 2 * A_SLOT + A_W + 8192), kw = rot * (64u * 2688u);
        dma2m<0, 896, 8 * 2688>(lwe, vw, rsW, kw, br ? ~0ull : wmask);
        dma2m<8192, 1792, 8 * 2688>(lwe, vw, rsW, kw, wmask);
        if (!br) {
            dma2m<-8192, 0, 8 * 2688>(lwe, vw, rsW, kw, wmask);
            const int hrow0 = ((xcd & 1) * 8 + tl) == 0 ? 0 : ((xcd & 1) * 8 + tl) * 128 - 16;
            const unsigned vh = (unsigned)((hrow0 + 4 * j + ((lane >> 3) & 3)) * 896 + (((lane & 7) ^ (2 * j + ((lane >> 4) & 1))) << 4));
            dma1m<2 * A_SLOT + A_HALO, 0>(lds0 + j * 512, vh, rsX, kx, 0xffffffffull);
        }
    }
    else {
        const int j = wq & 3, rs7 = ((bq >> 3) & 7) + ((bq >> 6) & 3) + (bq & 7), k0 = rs7 + (j < 3 ? j : 2), k1 = rs7 + 3 + j;
        gword = ld_sc1_32(__builtin_amdgcn_make_buffer_rsrc((void*)ws, 0, WS_END, 0x00020000), 0u, WS_G);
        gate0 = gw[(k0 - 7 * ((k0 * 37) >> 8)) * 64 + lq];
        gate1 = gw[(k1 - 7 * ((k1 * 37) >> 8)) * 64 + lq];
    }
    asm volatile("" : "+s"(bq), "+s"(wq) :: "memory");
    asm volatile("" : "+v"(lq));
    const int lane = lq & 63, w = wq & 7, b = bq & 255;
    const int xcd = b & 7, rr = b >> 3, tl = rr & 7, ul = rr >> 3;
    const int br = xcd < 4 ? 1 : 0, t = (xcd & 1) * 8 + tl, hp = ((xcd >> 1) & 1) * 4 + ul, u = br * 8 + hp;
    const bool isH = w >= 4;
    const int j = w & 3, hd = j & 1, th = j >> 1;
    const int r = lane & 31, hh = lane >> 5;
    const int q4 = (lane >> 2) & 3, p4 = lane & 3, blk = (lane >> 4) & 1;
    const int rot = (tl + ul + xcd) % 7;
    asm volatile("" :: "s"(x), "s"(wA), "s"(woA), "s"(wB), "s"(wphi), "s"(woB), "s"(gw), "s"(out), "s"(ws));

    const rsrc_t rsS = __builtin_amdgcn_make_buffer_rsrc((void*)ws, 0, WS_END, 0x00020000);
    auto kc_of = [&](int s) { int k = s + rot; return k >= 7 ? k - 7 : k; };
    const unsigned gpos = (unsigned)(lane * 128 + ((7 ^ (((lane >> 1) & 1) << 2)) << 4));
    auto wfrag_off = [&](int h2) {
        const int col = (blk == 1 && p4 == 3) ? (h2 ? 112 : 120) : h2 * 56 + blk * 32 + p4 * 8;
        return (unsigned)((8 * hh + q4) * 128 + (col ^ ((q4 >> 1) << 6)));
    };
    const bool do_out = br || rr >= 8;
    const int oi = br ? xcd * 32 + rr : do_out ? 24 * xcd + 24 + rr : 0;
    const int tg64 = oi / 7, ct = oi - tg64 * 7;
    const int wbase = br * B_WO;
    LAS char* fH = lds + B_FLG;
    LAS char* fC = fH + 16;
    LAS char* fL = fH + 48;
    auto nohook = [](auto) {};
    const int rw = (xcd - 4) * 32 + rr * 4 + j;
    const rsrc_t rsOA = __builtin_amdgcn_make_buffer_rsrc((void*)woA, 0, 448 * 896, 0x00020000);
    const rsrc_t rsOB = __builtin_amdgcn_make_buffer_rsrc((void*)woB, 0, 448 * 896, 0x00020000);
    const int cw = (lane & 7) ^ (((lane >> 4) & 1) << 2);
    const unsigned vo = (unsigned)((16 * j + (lane >> 3)) * 896 + ct * 128 + (cw << 4));
    const unsigned lw = lds0 + j * 2048;
    const unsigned szero = 0;
    auto dma_wo = [&](auto N_, auto L_) {
        constexpr int n = decltype(N_)::value, L = decltype(L_)::value;
        if constexpr (n < 7) dma2<L + n * 8192, n * 64 * 896, 8 * 896>(lw, vo, vo, rsOA, szero);
        else dma2<L + n * 8192, (n - 7) * 64 * 896, 8 * 896>(lw, vo, vo, rsOB, szero);
    };

    f32x16 zero16;
#pragma unroll
    for (int i = 0; i < 16; ++i) zero16[i] = 0.f;
    unsigned tag;

    unsigned fLa = lds0 + B_FLG + 48, one = 1u;
    auto kv_phase = [&](auto HD_, u32x4 (&fx)[28], auto&& at_stage, auto&& hook) {
        constexpr int h2 = decltype(HD_)::value;
        unsigned xo[4];
#pragma unroll
        for (int ks = 0; ks < 4; ++ks) xo[ks] = (unsigned)((j * 32 + r) * 128 + (((2 * ks + hh) ^ ((r >> 1) & 7)) << 4));
        LAS char* wk = lds + B_WK + (8 * hh + q4) * 128 + ((h2 * 56 + blk * 32 + p4 * 8) ^ ((q4 >> 1) << 6));
        LAS char* wv = lds + B_WV + wfrag_off(h2);
        LAS char* wk2 = wk + 2 * B_SLOT;
        LAS char* wv2 = wv + 2 * B_SLOT;
        LAS char* vb = lds + B_VF + h2 * 8192 + j * 2048 + lane * 16;
        LAS char* pw = lds + B_PHI + h2 * 16384 + (j * 32 + r) * 64;
        f32x16 ak = zero16, av;
        const float one28 = r == 28 ? 1.f : 0.f;
#pragma unroll
        for (int i = 0; i < 16; ++i) av[i] = one28;
        pin(wk); pin(wv); pin(wk2); pin(wv2); pin(vb); pin(fLa); pin(one);
        u32x4 fk[4], fv[4], wp0, wp1;
        sfor<0, 8>([&](auto S_) {
            constexpr int S = decltype(S_)::value;
            constexpr int SL = (S % 3) * B_SLOT;
            auto rd = [&](auto K_) {
                constexpr int ks = decltype(K_)::value;
                if constexpr (S < 7) {
                    LAS char* pk = (S % 3 == 2 ? wk2 : wk + SL) + ks * 2048;
                    LAS char* pv = (S % 3 == 2 ? wv2 : wv + SL) + ks * 2048;
                    fx[S * 4 + ks] = *(LAS u32x4*)(lds + SL + xo[ks]);
                    fk[ks] = tr_rd2(pk, pk + 512);
                    fv[ks] = tr_rd2(pv, pv + 512);
                    fence_sched();
                }
            };
            auto mm = [&](auto I_) {
                constexpr int i = decltype(I_)::value;
                ak = mfma(fk[i & 3], fx[i], ak);
                av = mfma(fx[i], fv[i & 3], av);
                fence_sched();
            };
            at_stage(S_, ic<0>{});
            rd(ic<0>{}); rd(ic<1>{});
            if constexpr (S > 0) mm(ic<4 * S - 2>{});
            at_stage(S_, ic<1>{});
            rd(ic<2>{});
            if constexpr (S > 0) mm(ic<4 * S - 1>{});
            at_stage(S_, ic<2>{});
            rd(ic<3>{});
            if constexpr (S < 7) mm(ic<4 * S>{});
            at_stage(S_, ic<3>{});
            if constexpr (S < 7) mm(ic<4 * S + 1>{});
            at_stage(S_, ic<4>{});
            if constexpr (S == 1) {
                const int fc = (blk == 1 && p4 == 3) ? 0 : (blk * 32 + p4 * 8);
                LAS char* pb = lds + B_PHIW + h2 * 1568 + (4 * hh + q4) * 56 + fc;
                wp0 = tr_rd2(pb, pb + 8 * 56);
                wp1 = tr_rd2(pb + 16 * 56, hh ? pb : pb + 24 * 56);
                if (hh) { wp1[2] = 0u; wp1[3] = 0u; }
                fence_sched();
            }
        });
        f32x16 p = mfma(wp0, pack_step<0>(ak), zero16);
        p = mfma(wp1, pack_step<1>(ak), p);
        const u32x4 v0 = pack_step<0>(av), v1 = pack_step<1>(av);
        *(LAS u32x4*)(vb) = v0;
        *(LAS u32x4*)(vb + 1024) = v1;
        fence_sched();
        unsigned fp[8], fn[8];
        phi_map(p, hh, fp, fn, hook);
        asm volatile("" :: "v"(v0), "v"(v1));
#pragma unroll
        for (int g = 0; g < 4; ++g) {
            const int so = ((2 * g + hh) ^ ((r >> 1) & 7)) << 3;
            *(LAS u32x2*)(pw + so) = (u32x2){fp[2 * g], fp[2 * g + 1]};
            *(LAS u32x2*)(pw + 8192 + so) = (u32x2){fn[2 * g], fn[2 * g + 1]};
        }
        cnt_add<4>(fLa, one);
    };

    const int tgl = j & 1, kh = j >> 1, ob = isH ? 0 : 1, kq = ob * 2 + kh;
    const int otg = tg64 * 2 + tgl;
    u32x4 wf[32];
    unsigned pre = 0u;
    auto load_wf = [&]() {
        const unsigned wob = (unsigned)(wbase + (kq * 224 + 4 * hh + q4) * 128);
        const unsigned woc0 = wob + (unsigned)((blk * 32 + p4 * 8) ^ ((q4 >> 1) << 6));
        const unsigned woc1 = woc0 ^ 64u;
        const unsigned pad = hh ? 20 * 128 : 24 * 128;
#pragma unroll
        for (int i = 0; i < 16; ++i) {
            LAS char* p = lds + ((i >> 1) * 28 + (i & 1) * 16) * 128;
            LAS char* p2 = (i & 1) ? lds + (i >> 1) * 28 * 128 + pad : p + 1024;
            wf[2 * i] = tr_rd2(p + woc0, p2 + woc0);
            wf[2 * i + 1] = tr_rd2(p + woc1, p2 + woc1);
        }
    };
    auto publish = [&](int grp, int h2, const u32x4& f0, const u32x4& f1) {
        const unsigned sb = WS_OF + (unsigned)(((((t * 4 + grp) * 16 + u) * 2 + h2) * 2) * 1024);
        st_sc1(f0, rsS, (unsigned)(lane * 16), sb);
        st_sc1(f1, rsS, (unsigned)(lane * 16), sb + 1024);
    };
    auto flag_of = [&](int grp, int h2) { return WS_FO + (unsigned)(((t * 4 + grp) * 4 + (u >> 2)) * 128 + ((u & 3) * 2 + h2) * 4); };
    auto q_phase = [&](auto HD_, const u32x4 (&fx)[28], auto&& at, auto&& hook, auto&& mid, LAS char* fw, unsigned code) {
        constexpr int h2 = decltype(HD_)::value;
        LAS char* wb = lds + B_WQ + wfrag_off(h2);
        const int fc = (blk == 1 && p4 == 3) ? 0 : (blk * 32 + p4 * 8);
        LAS char* pb = lds + B_PHIW + h2 * 1568 + (4 * hh + q4) * 56 + fc;
        pin(wb); pin(pb);
        u32x4 wq[8], wp0, wp1;
        auto rd = [&](auto I_) {
            constexpr int i = decltype(I_)::value;
            if constexpr (i < 28) wq[i & 7] = tr_rd2(wb + i * 2048, wb + i * 2048 + 512);
            if constexpr (i == 7) cnt_add<8>(fLa, one);
            if constexpr (i == 28) wp0 = tr_rd2(pb, pb + 8 * 56);
            if constexpr (i == 29) wp1 = tr_rd2(pb + 16 * 56, hh ? pb : pb + 24 * 56);
            fence_sched();
        };
        at(ic<-1>{});
        sfor<0, QPD>(rd);
        f32x16 a;
        sfor<0, 28>([&](auto I_) {
            constexpr int i = decltype(I_)::value;
            at(I_);
            rd(ic<i + QPD>{});
            a = mfma(wq[i & 7], fx[i], i == 0 ? zero16 : a);
            fence_sched();
        });
        if (hh) { wp1[2] = 0u; wp1[3] = 0u; }
        f32x16 p = mfma(wp0, pack_step<0>(a), zero16);
        p = mfma(wp1, pack_step<1>(a), p);
        float g1 = __builtin_amdgcn_rcpf(1.f + __builtin_amdgcn_exp2f(a[12] * LOG2E));
        unsigned fp[8], fn[8];
        phi_map(p, hh, fp, fn, hook);
        u32x4 pq[4] = {{fp[0], fp[1], fp[2], fp[3]}, {fp[4], fp[5], fp[6], fp[7]}, {fn[0], fn[1], fn[2], fn[3]}, {fn[4], fn[5], fn[6], fn[7]}};
        pin(pq[0]); pin(pq[1]); pin(pq[2]); pin(pq[3]); pin(g1);
        mid();
        u32x16 gq[1];
        fetch4<STG2>(gq, rsS, (unsigned)(lane * 16), WS_L2 + (unsigned)(hp * 8192 + h2 * 4096));
        f32x16 o = mfma(quad<0>(gq[0]), pq[0], zero16);
        o = mfma(quad<1>(gq[0]), pq[1], o);
        o = mfma(quad<2>(gq[0]), pq[2], o);
        o = mfma(quad<3>(gq[0]), pq[3], o);
        const float sc = from_hi(g1 * __builtin_amdgcn_rcpf(o[12] + 1e-6f));
        u32x4 f0, f1;
        scale_pack(o, sc, hh, f0, f1);
        publish(j, h2, f0, f1);
        flg_set(fw, code);
        wait_vm<0>();
        if (lane == 0) st_sc1_32(tag, rsS, 0u, flag_of(j, h2));
    };

    if (isH) {
        const rsrc_t rsX = __builtin_amdgcn_make_buffer_rsrc((void*)x, 0, 2048 * 896, 0x00020000);
        const rsrc_t rsW = __builtin_amdgcn_make_buffer_rsrc((void*)(br ? wB : wA), 0, 448 * 2688, 0x00020000);
        const unsigned vxr = (unsigned)((t * 128 + 32 * j + (lane >> 3)) * 896);
        const unsigned vx0 = vxr + (((lane & 7) ^ (lane >> 4)) << 4), vx1 = vxr + (((lane & 7) ^ ((lane >> 4) + 4)) << 4);
        const unsigned vw = (unsigned)((16 * j + (lane >> 3)) * 2688 + hp * 112 + ((cw == 7 ? 6 : cw) << 4));
        const unsigned lx = lds0 + j * 4096;
        const unsigned long long wmask = ~0x0808808008088080ull;
        LAS char* fme = fH + j * 4;
        auto dma_x = [&](auto L_, unsigned kx) {
            constexpr int L = decltype(L_)::value;
            dma2<L, 0, 8 * 896>(lx, vx0, vx1, rsX, kx);
            dma2<L + 2048, 16 * 896, 8 * 896>(lx, vx0, vx1, rsX, kx);
        };
        if (br == 0) {
            __builtin_amdgcn_s_setprio(2);
            const int hrow0 = t == 0 ? 0 : t * 128 - 16;
            const unsigned vh = (unsigned)((hrow0 + 4 * j + ((lane >> 3) & 3)) * 896 + (((lane & 7) ^ (2 * j + ((lane >> 4) & 1))) << 4));
            const unsigned lh = lds0 + j * 512;
            auto issue = [&](auto S_, auto N_) {
                constexpr int s = decltype(S_)::value, n = decltype(N_)::value;
                constexpr int sl = ((s + 2) % 3) * A_SLOT;
                const unsigned kx = (unsigned)kc_of(s) * 128u, kw = (unsigned)kc_of(s) * (64u * 2688u);
                if constexpr (n == 0) { dma_x(ic<sl>{}, kx); dma1m<sl + A_HALO, 0>(lh, vh, rsX, kx, 0xffffffffull); }
                if constexpr (n == 1) dma2m<sl + A_W, 0, 8 * 2688>(lw, vw, rsW, kw, wmask);
                if constexpr (n == 2) dma2m<sl + A_W + 8192, 896, 8 * 2688>(lw, vw, rsW, kw, wmask);
                if constexpr (n == 3) dma2m<sl + A_W + 16384, 1792, 8 * 2688>(lw, vw, rsW, kw, wmask);
            };
            auto woa = [&](auto N_) { dma_wo(N_, ic<0>{}); };
            sfor<0, 4>([&](auto N_) { issue(ic<1>{}, N_); });
            const int g16 = lane >> 4, l16 = lane & 15;
            unsigned hxo[2], hwo[2];
#pragma unroll
            for (int kk = 0; kk < 2; ++kk) hxo[kk] = (unsigned)(A_HALO + l16 * 128 + (((4 * kk + g16) ^ (l16 >> 1)) << 4));
#pragma unroll
            for (int bk = 0; bk < 2; ++bk) {
                const int col = (bk == 1 && p4 == 3) ? (hd ? 112 : 120) : hd * 56 + bk * 32 + p4 * 8;
                hwo[bk] = (unsigned)(A_W + (1 + th) * 8192 + (8 * g16 + q4) * 128 + (col ^ ((q4 >> 1) << 6)));
            }
            const float one28 = (th == 1 && l16 == 12) ? 1.f : 0.f;
            f32x4 ha0 = {0.f, 0.f, 0.f, 0.f}, ha1 = {one28, one28, one28, one28};
            unsigned gate1;
            sfor<0, 7>([&](auto S_) {
                constexpr int S = decltype(S_)::value;
                constexpr int SL = ((S + 2) % 3) * A_SLOT;
                if constexpr (S < 6) wait_vm_bar<11>();
                else { if (do_out) wait_vm_bar<8>(); else wait_vm_bar<0>(); }
                if constexpr (S == 0) asm volatile("" :: "v"(cpf));
                if constexpr (S <= 4) issue(ic<S + 2>{}, ic<0>{});
                if constexpr (S == 5) { if (do_out) sfor<0, 2>(woa); }
                fence_sched();
                u32x4 hx[2], hw[2][2];
#pragma unroll
                for (int kk = 0; kk < 2; ++kk) {
                    hx[kk] = *(LAS u32x4*)(lds + SL + hxo[kk]);
#pragma unroll
                    for (int bk = 0; bk < 2; ++bk) hw[kk][bk] = tr_rd2(lds + SL + hwo[bk] + kk * 4096, lds + SL + hwo[bk] + kk * 4096 + 512);
                }
                if constexpr (S >= 1 && S <= 4) {
                    if (j == S - 1) *(LAS u32x4*)(lds + ((S + 4) % 3) * A_SLOT + A_W + gpos) = (u32x4){gate1, 0u, gate1, 0u};
                }
                if constexpr (S == 0) {
                    gate1 = *(LAS unsigned*)(lds + G_STASH + (3 + j) * 256 + lane * 4);
                    tag = __builtin_amdgcn_readfirstlane(*(LAS unsigned*)(lds + G_TAG));
                }
                fence_sched();
                if constexpr (S <= 4) { issue(ic<S + 2>{}, ic<1>{}); issue(ic<S + 2>{}, ic<2>{}); issue(ic<S + 2>{}, ic<3>{}); }
                if constexpr (S == 5) { if (do_out) sfor<2, 4>(woa); }
                fence_sched();
                if (th == 0) {
#pragma unroll
                    for (int kk = 0; kk < 2; ++kk) { ha0 = mfma16(hw[kk][0], hx[kk], ha0); ha1 = mfma16(hw[kk][1], hx[kk], ha1); }
                } else {
#pragma unroll
                    for (int kk = 0; kk < 2; ++kk) { ha0 = mfma16(hx[kk], hw[kk][0], ha0); ha1 = mfma16(hx[kk], hw[kk][1], ha1); }
                }
                fence_sched();
            });
            {
                LAS char* xb = lds + A_XCH + hd * 6144 + (32 * (g16 & 1) + l16) * 16 + (g16 >> 1) * 8;
                const u32x2 p0 = {pk2(ha0[0], ha0[1]), pk2(ha0[2], ha0[3])}, p1 = {pk2(ha1[0], ha1[1]), pk2(ha1[2], ha1[3])};
                *(LAS u32x2*)(xb + (th ? 2048 : 256)) = p0;
                *(LAS u32x2*)(xb + (th ? 2048 + 256 : 1024 + 256)) = p1;
            }
            flg_set(fme, 1);
            if (do_out) {
                sfor<4, 10>(woa);
                flg_wait(fC, 1, flg_rd(fC));
                flg_wait(fH, 1, flg_rd(fH));
                sfor<10, 14>(woa);
                wait_vm<0>();
                flg_set(fme, H_WO);
            } else {
                u32x16 gq[2];
                fetch8<STG1>(gq, rsS, (unsigned)(lane * 16), WS_L1 + (unsigned)(rw * 8192));
                const u32x4 gr[8] = {quad<0>(gq[0]), quad<1>(gq[0]), quad<2>(gq[0]), quad<3>(gq[0]), quad<0>(gq[1]), quad<1>(gq[1]), quad<2>(gq[1]), quad<3>(gq[1])};
                float sv[8];
#pragma unroll
                for (int i = 0; i < 4; ++i) {
                    sv[2 * i] = __uint_as_float(gr[0][i] << 16);
                    sv[2 * i + 1] = __uint_as_float(gr[0][i] & 0xffff0000u);
#pragma unroll
                    for (int n = 1; n < 8; ++n) {
                        sv[2 * i] += __uint_as_float(gr[n][i] << 16);
                        sv[2 * i + 1] += __uint_as_float(gr[n][i] & 0xffff0000u);
                    }
                }
#pragma unroll
                for (int i = 0; i < 4; ++i) {
                    auto e = __builtin_amdgcn_permlane32_swap(__float_as_uint(sv[i]), __float_as_uint(sv[i + 4]), false, false);
                    sv[i] = __uint_as_float(e[0]) + __uint_as_float(e[1]);
                }
                st_sc1_64((u32x2){umin(pk2(sv[0], sv[1]), EMPTY - 1u), umin(pk2(sv[2], sv[3]), EMPTY - 1u)}, rsS, (unsigned)(r * 16 + hh * 8), WS_L2 + (unsigned)(rw * 512));
                if (RDM == 2) wait_vm<0>();
                flg_set(fme, 2);
                wait_vm<0>();
            }
        } else {
            const rsrc_t rsP = __builtin_amdgcn_make_buffer_rsrc((void*)wphi, 0, 16 * 1568, 0x00020000);
            auto issue = [&](auto S_, auto N_) {
                constexpr int s = decltype(S_)::value, n = decltype(N_)::value;
                constexpr int sl = (s % 3) * B_SLOT;
                const unsigned kx = (unsigned)kc_of(s) * 128u, kw = (unsigned)kc_of(s) * (64u * 2688u);
                if constexpr (n == 0) dma2<sl, 0, 8 * 896>(lx, vx0, vx1, rsX, kx);
                if constexpr (n == 1) dma2<sl + 2048, 16 * 896, 8 * 896>(lx, vx0, vx1, rsX, kx);
                if constexpr (n == 2) dma2<sl + B_WK, 896, 8 * 2688>(lw, vw, vw, rsW, kw);
                if constexpr (n == 3) dma2m<sl + B_WV, 1792, 8 * 2688>(lw, vw, rsW, kw, wmask);
            };
            auto issue_q = [&](auto D_) {
                constexpr int n = decltype(D_)::value >> 1, h = decltype(D_)::value & 1;
                dma1m<B_WQ + n * 8192 + h * 1024, h * 8 * 2688>(lw, vw, rsW, (unsigned)kc_of(n) * (64u * 2688u), wmask);
            };
            {
                const int c = j * 64 + lane;
                dma1<B_PHIW, 0>(lds0 + j * 1024, (unsigned)((c < 196 ? c : 195) * 16 + hp * 3136), rsP, szero);
            }
            sfor<0, 4>([&](auto N_) { issue(ic<1>{}, N_); });
            u32x4 xf[28];
            const unsigned char* pfp = (const unsigned char*)(rr < 16 ? woA : woB) + (((rr * 4 + j) & 63) * 49 + (lane < 49 ? lane : 48)) * 128;
            pin(pfp);
            unsigned pfv, fl;
            kv_phase(ic<1>{}, xf, [&](auto S_, auto P_) {
                constexpr int S = decltype(S_)::value, P = decltype(P_)::value;
                if constexpr (P == 0 && S < 7) {
                    wait_vm_bar<S == 0 ? 9 : S < 6 ? 8 : Q5>();
                    if constexpr (S == 0) asm volatile("" :: "v"(cpf));
                }
                if constexpr (P == 4 && S == 0) tag = __builtin_amdgcn_readfirstlane(*(LAS unsigned*)(lds + G_TAG));
                if constexpr (P > 0 && S <= 4) issue(ic<S + 2>{}, ic<P - 1>{});
                if constexpr (P > 0 && S == 5) sfor<(P - 1) * Q5 / 4, P * Q5 / 4>(issue_q);
                if constexpr (P > 0 && S == 6) sfor<Q5 + (P - 1) * Q6 / 4, Q5 + P * Q6 / 4>(issue_q);
                fence_sched();
            }, [&](auto K_) {
                constexpr int d = Q5 + Q6 + decltype(K_)::value;
                if constexpr (d <= 14) {
                    fence_sched();
                    if constexpr (d < 14) issue_q(ic<d>{});
                    else pfv = touch(pfp);
                    fence_sched();
                }
            });
            q_phase(ic<1>{}, xf, [&](auto I_) {
                constexpr int i = decltype(I_)::value;
                if constexpr (i == -1) {
                    __builtin_amdgcn_s_setprio(HPRIO);
                    wait_vm<7>();
                    cnt_add(fLa, one);
                    cnt_wait<4>(fL, cnt_rd((LAS char*)(uintptr_t)fLa));
                }
                if constexpr (i == 12 - QPD) { wait_vm<1>(); cnt_add(fLa, one); }
                if constexpr (i == 14 - QPD) fl = cnt_rd((LAS char*)(uintptr_t)fLa);
                if constexpr (i == 16 - QPD) cnt_wait<8>(fL, fl);
                fence_sched();
            }, nohook, [&]() {
                __builtin_amdgcn_s_setprio(0);
                flg_wait(fC, 3, flg_rd(fC));
                sfor<NCW, 14>([&](auto M_) {
                    constexpr int m = decltype(M_)::value;
                    if constexpr (m == 12) cnt_wait<8>(fL + 8, cnt_rd(fL + 8));
                    dma_wo(M_, ic<B_WO>{});
                });
                flg_wait(fC, 4, flg_rd(fC));
                load_wf();
#pragma unroll
                for (int i = 0; i < 32; ++i) pin(wf[i]);
            }, fme, H_WO);
            asm volatile("" :: "v"(pfv));
        }
    } else {
        const u32x4 zero4 = {0u, 0u, 0u, 0u};
        if (j == 3 && lane < 4) *(LAS u32x4*)(lds + B_FLG + lane * 16) = zero4;
        auto wr_gate = [&]() {
            tag = __builtin_amdgcn_readfirstlane(gword) + 1u;
            asm volatile("" :: "v"(cpf), "s"(tag));
            if (j < 3) {
                if (br == 0) *(LAS u32x4*)(lds + ((j + 2) % 3) * A_SLOT + A_W + gpos) = (u32x4){gate0, 0u, gate0, 0u};
                else *(LAS u32x4*)(lds + B_WQ + j * 8192 + gpos) = (u32x4){gate0, 0u, gate0, 0u};
            } else if (lane == 0) *(LAS unsigned*)(lds + G_TAG) = tag;
            if (br == 0) *(LAS unsigned*)(lds + G_STASH + (3 + j) * 256 + lane * 4) = gate1;
            else *(LAS u32x4*)(lds + B_WQ + (3 + j) * 8192 + gpos) = (u32x4){gate1, 0u, gate1, 0u};
        };
        auto started = [&]() { if (j == 0 && lane == 0) st_sc1_32(tag, rsS, 0u, WS_START + (unsigned)b * 4u); };
        unsigned xoff[4];
#pragma unroll
        for (int ks = 0; ks < 4; ++ks) xoff[ks] = (unsigned)((th * 64 + r) * 128 + (((2 * ks + hh) ^ ((r >> 1) & 7)) << 4));
        if (br == 0) {
            const unsigned wfo = wfrag_off(hd);
            const int cl = ((xcd - 4) * 24 + rr - 8) * 4 + j, cls = cl / 3, clp = cl - cls * 3;
            unsigned fc = 0u;
            auto clear = [&]() {
                if (do_out) {
                    const u32x4 e4 = {EMPTY, EMPTY, EMPTY, EMPTY};
                    const unsigned sb = WS_L1 + (unsigned)(cls * 8192 + clp * 3072);
                    st_sc1(e4, rsS, (unsigned)(lane * 16), sb);
                    st_sc1(e4, rsS, (unsigned)(lane * 16), sb + 1024);
                    st_sc1(e4, rsS, (unsigned)((clp == 2 ? r : lane) * 16), clp == 2 ? WS_L2 + (unsigned)(cls * 512) : sb + 2048);
                }
            };
            *(LAS u32x4*)(lds + (j >> 1) * A_SLOT + A_W + 8192 + (j & 1) * 8192 + gpos) = zero4;
            if (j < 2) *(LAS u32x4*)(lds + 2 * A_SLOT + A_W + 8192 + j * 8192 + gpos) = zero4;
            f32x16 mso, msp, msp0;
            const bool noprev = (t == 0) && (th == 0);
#pragma unroll
            for (int i = 0; i < 16; ++i) {
                const int d = r - (8 * (i >> 2) + 4 * hh + (i & 3));
                mso[i] = ((unsigned)d <= 14u && !(d & 1)) ? 0.f : -__builtin_inff();
                msp[i] = (i >= 8 && !((unsigned)(d + 32) <= 14u && !(d & 1))) ? -__builtin_inff() : 0.f;
                msp0[i] = (i >= 8 && noprev) ? -__builtin_inff() : msp[i];
            }
            asm volatile("" : "+v"(mso), "+v"(msp), "+v"(msp0));
            f32x16 aq[2] = {zero16, zero16}, ak[2] = {zero16, zero16}, av[2];
            {
                const float one28 = r == 28 ? 1.f : 0.f;
#pragma unroll
                for (int i = 0; i < 16; ++i) av[0][i] = one28;
                av[1] = av[0];
            }
            wr_gate();
            u32x4 fr[2][5];
            auto rd = [&](auto I_, auto F_) {
                constexpr int i = decltype(I_)::value, f = decltype(F_)::value, SL = (((i >> 2) + 2) % 3) * A_SLOT, ks = i & 3;
                if constexpr (i < 28) {
                    if constexpr (f < 2) fr[i & 1][f] = *(LAS u32x4*)(lds + SL + f * 4096 + xoff[ks]);
                    else {
                        LAS char* wb = lds + SL + A_W + (f - 2) * 8192 + ks * 2048 + wfo;
                        fr[i & 1][f] = tr_rd2(wb, wb + 512);
                    }
                }
                fence_sched();
            };
            bar_lds();
            started();
            clear();
            rd(ic<0>{}, ic<0>{}); rd(ic<0>{}, ic<2>{}); rd(ic<0>{}, ic<1>{}); rd(ic<0>{}, ic<3>{}); rd(ic<0>{}, ic<4>{});
            sfor<0, 28>([&](auto I_) {
                constexpr int i = decltype(I_)::value;
                constexpr bool last = (i & 3) == 3;
                const u32x4(&f)[5] = fr[i & 1];
                auto N = ic<i + 1>{};
                aq[0] = mfma(f[2], f[0], aq[0]);
                fence_sched();
                if constexpr (!last) { rd(N, ic<0>{}); rd(N, ic<2>{}); }
                aq[1] = mfma(f[2], f[1], aq[1]);
                fence_sched();
                if constexpr (last && i < 27) {
                    if constexpr (i == CLR_I) {
                        if (do_out) {
                            wait_vm<0>();
                            if (lane == 0) st_sc1_32(tag, rsS, 0u, WS_FR + (unsigned)(cl * 4));
                        }
                    }
                    if constexpr (i == 19) {
                        if (!do_out) fc = ld_sc1_32(rsS, (unsigned)((lane < 2 ? lane : 2) * 4), WS_FR + (unsigned)(rw * 12));
                    }
                    if constexpr (i == 23) {
                        if (!do_out) {
                            if (__builtin_expect(__builtin_amdgcn_ballot_w64(fc != tag) != 0ull, 0)) wait_flags<3>(rsS, (unsigned)(lane * 4), WS_FR + (unsigned)(rw * 12), tag);
                        }
                    }
                    bar_lds();
                    rd(N, ic<0>{}); rd(N, ic<2>{});
                } else rd(N, ic<1>{});
                ak[1] = mfma(f[3], f[1], ak[1]);
                fence_sched();
                if constexpr (last) rd(N, ic<1>{});
                rd(N, ic<3>{});
                av[1] = mfma(f[1], f[4], av[1]);
                fence_sched();
                rd(N, ic<4>{});
                ak[0] = mfma(f[3], f[0], ak[0]);
                av[0] = mfma(f[0], f[4], av[0]);
                fence_sched();
            });
            u32x4 qf[2][2], kf[2][2], vf[2][2];
            kf[1][0] = pack_step<0>(ak[1]); kf[1][1] = pack_step<1>(ak[1]); vf[1][1] = pack_step<1>(av[1]);
            if (th == 0) {
                LAS char* xb = lds + A_XCH + hd * 6144 + 3072 + lane * 16;
                *(LAS u32x4*)(xb) = kf[1][0];
                *(LAS u32x4*)(xb + 1024) = kf[1][1];
                *(LAS u32x4*)(xb + 2048) = vf[1][1];
            }
            flg_set(fC + j * 4, 1);
            qf[1][0] = pack_step<0>(aq[1]); qf[1][1] = pack_step<1>(aq[1]);
            kf[0][0] = pack_step<0>(ak[0]); kf[0][1] = pack_step<1>(ak[0]);
            f32x16 so[2], sp[2];
            so[1] = mfma(kf[1][0], qf[1][0], mso);
            sp[1] = mfma(kf[0][0], qf[1][0], msp);
            so[1] = mfma(kf[1][1], qf[1][1], so[1]);
            sp[1] = mfma(kf[0][1], qf[1][1], sp[1]);
            qf[0][0] = pack_step<0>(aq[0]); qf[0][1] = pack_step<1>(aq[0]);
            so[0] = mfma(kf[0][0], qf[0][0], mso);
            so[0] = mfma(kf[0][1], qf[0][1], so[0]);
            fence_sched();
            flg_wait(th ? fC : fH, 1, flg_rd(th ? fC : fH));
            u32x4 pk0, pk1, pv1;
            {
                LAS char* xb = lds + A_XCH + hd * 6144 + th * 3072 + lane * 16;
                pk0 = *(LAS u32x4*)(xb); pk1 = *(LAS u32x4*)(xb + 1024); pv1 = *(LAS u32x4*)(xb + 2048);
            }
            vf[0][0] = pack_step<0>(av[0]); vf[0][1] = pack_step<1>(av[0]); vf[1][0] = pack_step<0>(av[1]);
            sp[0] = mfma(pk0, qf[0][0], msp0);
            sp[0] = mfma(pk1, qf[0][1], sp[0]);
            const unsigned selL = (r & 1) ? 0x01000c0cu : 0x0c0c0100u, selH = (r & 1) ? 0x03020c0cu : 0x0c0c0302u;
            const float SC = 0.18898223650461362f * LOG2E;
            u32x4 po[2][2];
            sfor<0, 2>([&](auto T_) {
                constexpr int T = 1 - decltype(T_)::value;
                float c[12];
#pragma unroll
                for (int m = 0; m < 8; ++m) c[m] = fmx(so[T][2 * m], so[T][2 * m + 1]);
#pragma unroll
                for (int m = 0; m < 4; ++m) c[8 + m] = fmx(sp[T][8 + 2 * m], sp[T][9 + 2 * m]);
                float mx = c[0];
#pragma unroll
                for (int m = 1; m < 12; ++m) mx = fmx(mx, c[m]);
                mx = pair_max(mx);
                const float bias = -mx * SC;
                unsigned pw[12];
#pragma unroll
                for (int m = 0; m < 6; ++m) {
                    const f32x2 a = __builtin_elementwise_fma((f32x2){c[2 * m], c[2 * m + 1]}, (f32x2){SC, SC}, (f32x2){bias, bias});
                    const unsigned e2 = pk2(__builtin_amdgcn_exp2f(a[0]), __builtin_amdgcn_exp2f(a[1]));
                    pw[2 * m] = __builtin_amdgcn_perm(e2, e2, selL);
                    pw[2 * m + 1] = __builtin_amdgcn_perm(e2, e2, selH);
                }
                f32x16 o = mfma(vf[T][0], (u32x4){pw[0], pw[1], pw[2], pw[3]}, zero16);
                o = mfma(vf[T][1], (u32x4){pw[4], pw[5], pw[6], pw[7]}, o);
                o = mfma(T ? vf[0][1] : pv1, (u32x4){pw[8], pw[9], pw[10], pw[11]}, o);
                const float sc = from_hi(__builtin_amdgcn_rcpf((1.f + __builtin_amdgcn_exp2f(-aq[T][12] * LOG2E)) * o[12]));
                scale_pack(o, sc, hh, po[T][0], po[T][1]);
                if (RDM == 0 || (RDM == 3 && T == 1) || do_out) publish(th * 2 + T, hd, po[T][0], po[T][1]);
            });
            if (RDM && !do_out) {
                flg_wait(fH, 2, flg_rd(fH));
                if (RDM != 3) publish(th * 2 + 1, hd, po[1][0], po[1][1]);
                publish(th * 2, hd, po[0][0], po[0][1]);
            }
            wait_vm<0>();
            if (lane < 2) st_sc1_32(tag, rsS, (unsigned)(lane * 512), flag_of(th * 2, hd));
        } else {
            if (j < 3) *(LAS u32x4*)(lds + j * B_SLOT + B_WV + gpos) = zero4;
            wr_gate();
            unsigned fr;
            u32x4 fx[28];
            {
                kv_phase(ic<0>{}, fx, [&](auto S_, auto P_) {
                    constexpr int S = decltype(S_)::value, P = decltype(P_)::value;
                    if constexpr (P == 0 && S < 7) {
                        bar_lds();
                        if constexpr (S == 0) started();
                    }
                }, [&](auto K_) {
                    if constexpr (decltype(K_)::value == FRK) { fence_sched(); fr = ld_sc1_32(rsS, (unsigned)((lane < 47 ? lane : 47) * 4), WS_FR + (unsigned)(hp * 192)); fence_sched(); }
                });
            }
            {
                LAS char* pb0 = lds + B_PHI + hd * 16384 + th * 8192 + (4 * hh + q4) * 64 + (((4 * blk + p4) ^ (2 * hh + (q4 >> 1))) << 3);
                LAS char* pb1 = lds + B_PHI + hd * 16384 + th * 8192 + (8 + 4 * hh + q4) * 64 + (((4 * blk + p4) ^ (4 + 2 * hh + (q4 >> 1))) << 3);
                u32x4 pa[8], vv[8];
                LAS char* vb = lds + B_VF + hd * 8192 + lane * 16;
                cnt_wait<8>(fL + 4, cnt_rd(fL + 4));
                __builtin_amdgcn_s_setprio(KPRIO);
#pragma unroll
                for (int i = 0; i < 8; ++i) { pa[i] = tr_rd2(pb0 + i * 1024, pb1 + i * 1024); vv[i] = *(LAS u32x4*)(vb + i * 1024); }
                fence_sched();
                if (__builtin_expect(__builtin_amdgcn_ballot_w64(fr != tag) != 0ull, 0)) wait_flags<64>(rsS, (unsigned)((lane < 47 ? lane : 47) * 4), WS_FR + (unsigned)(hp * 192), tag);
                f32x16 kv = zero16;
#pragma unroll
                for (int i = 0; i < 8; ++i) kv = mfma(pa[i], vv[i], kv);
                fence_sched();
                flg_set(fC + j * 4, 3);
                const unsigned v1 = (unsigned)(hh * 8192 + r * 16), sb1 = WS_L1 + (unsigned)((hp * 16 + (hd * 2 + th) * 4) * 8192 + t * 512);
                st_sc1(payload(pack_step<0>(kv)), rsS, v1, sb1);
                st_sc1(payload(pack_step<1>(kv)), rsS, v1, sb1 + 16384);
            }
            __builtin_amdgcn_s_setprio(0);
            sfor<0, NCW>([&](auto M_) {
                constexpr int m = decltype(M_)::value;
                if constexpr (m == 4) flg_wait(fC, 3, flg_rd(fC));
                dma_wo(M_, ic<B_WO>{});
            });
            q_phase(ic<0>{}, fx, [&](auto I_) {
                if constexpr (decltype(I_)::value == -1) cnt_wait<8>(fL, cnt_rd((LAS char*)(uintptr_t)fLa));
                if constexpr (decltype(I_)::value == CWI) { wait_vm<0>(); flg_set(fC + j * 4, 4); fence_sched(); }
            }, nohook, [&]() {}, fC + j * 4, 4);
            flg_set(fC + j * 4, 5);
        }
    }

    if (do_out) {
        if (!(isH && br)) flg_wait(fH, H_WO, flg_rd(fH));
        if (!isH && br) flg_wait(fC, 4, flg_rd(fC));
        u32x4 of[16];
        if (!(isH && br)) load_wf();
        if (isH) {
            flg_set(fH + j * 4, H_WF);
        }
        wait_flags<8>(rsS, (unsigned)(lane * 4), WS_FO + (unsigned)((otg * 4 + kq) * 128), tag);
        if (isH && br) { flg_wait(fC, 5, flg_rd(fC)); flg_wait(fH, H_WF, flg_rd(fH)); }
#pragma unroll
        for (int i = 0; i < 16; ++i) of[i] = ld_sc1(rsS, (unsigned)(lane * 16), WS_OF + (unsigned)((otg * 16 + kq * 4) * 4096) + i * 1024);
        const u32x4 fl = flg_rd(fH);
        fence_sched();
        f32x16 acc0 = zero16, acc1 = zero16;
#pragma unroll
        for (int i = 0; i < 16; ++i) { acc0 = mfma(wf[2 * i], of[i], acc0); acc1 = mfma(wf[2 * i + 1], of[i], acc1); }
        flg_wait(fH, H_WF, fl);
        {
            LAS char* rb = lds + wbase + tgl * 24576 + lane * 16;
#pragma unroll
            for (int qd = 0; qd < 8; ++qd) {
                const int own = qd >> 1;
                const f32x16& a = qd < 4 ? acc0 : acc1;
                const int g = qd & 3;
                const f32x4 v = {a[4 * g], a[4 * g + 1], a[4 * g + 2], a[4 * g + 3]};
                if (own != kq) {
                    const int src = kq - (kq > own ? 1 : 0);
                    *(LAS f32x4*)(rb + own * 6144 + src * 2048 + (qd & 1) * 1024) = v;
                }
            }
            bar_lds();
            f32x4 v0, v1;
            {
                const f32x16& a = kq < 2 ? acc0 : acc1;
                if (kq & 1) { v0 = (f32x4){a[8], a[9], a[10], a[11]}; v1 = (f32x4){a[12], a[13], a[14], a[15]}; }
                else { v0 = (f32x4){a[0], a[1], a[2], a[3]}; v1 = (f32x4){a[4], a[5], a[6], a[7]}; }
            }
#pragma unroll
            for (int s = 0; s < 3; ++s) {
                v0 += *(LAS f32x4*)(rb + kq * 6144 + s * 2048);
                v1 += *(LAS f32x4*)(rb + kq * 6144 + s * 2048 + 1024);
            }
            const unsigned a0 = pk2(v0[0], v0[1]), a1 = pk2(v0[2], v0[3]), b0 = pk2(v1[0], v1[1]), b1 = pk2(v1[2], v1[3]);
            auto s0 = __builtin_amdgcn_permlane32_swap(a0, b0, false, false);
            auto s1 = __builtin_amdgcn_permlane32_swap(a1, b1, false, false);
            const u32x4 ov = {s0[0], s1[0], s0[1], s1[1]};
            const rsrc_t rsD = __builtin_amdgcn_make_buffer_rsrc((void*)out, 0, 2048 * 896, 0x00020000);
            __builtin_amdgcn_raw_buffer_store_b128(ov, rsD, (unsigned)(((otg * 32 + r) * 448 + ct * 64 + kq * 16 + hh * 8) * 2), 0u, 2);
        }
    } else if (b == 4 && w == 0) {
        for (int spin = 0; spin < SPIN_CAP; ++spin) {
            asm volatile("" ::: "memory");
            const u32x4 f = ld_sc1(rsS, (unsigned)(lane * 16), WS_START);
            if (__all((f[0] == tag) & (f[1] == tag) & (f[2] == tag) & (f[3] == tag))) break;
            __builtin_amdgcn_s_sleep(16);
        }
        if (lane == 0) st_sc1_32(tag, rsS, 0u, WS_G);
    }
}

extern "C" void kernel_launch(void* const* d_in, const int* in_sizes, int n_in, void* d_out, int out_size, void* d_ws, size_t ws_size,
                              hipStream_t stream) {
    fused_kernel<<<256, 512, 0, stream>>>((const unsigned short*)d_in[0], (const unsigned short*)d_in[1], (const unsigned short*)d_in[2],
                                          (const unsigned short*)d_in[4], (const unsigned short*)d_in[5], (const unsigned short*)d_in[6],
                                          (const unsigned short*)d_in[7], (unsigned short*)d_out, (unsigned char*)d_ws);
}
```
